```python
import torch

def _softmax(x, axis=-1):
    return torch.softmax(x, dim=axis)
def _gelu(x):
    return torch.nn.functional.gelu(x, approximate="tanh")
def _silu(x):
    return torch.nn.functional.silu(x)
def _sigmoid(x):
    return torch.sigmoid(x)
def _layernorm(x, axis=-1, eps=1e-5):
    m = x.mean(dim=axis, keepdim=True)
    v = x.var(dim=axis, keepdim=True, unbiased=False)
    return (x - m) / torch.sqrt(v + eps)
def _scatter_add(tgt, idx, src, axis=0):
    return tgt.clone().index_add_(axis, idx, src)
def _segment_sum(src, seg, n):
    out = torch.zeros((n,) + src.shape[1:], dtype=src.dtype)
    return out.index_add_(0, seg, src)
def _pad(x, pw):
    flat = tuple(v for pair in reversed(pw) for v in pair)
    return torch.nn.functional.pad(x, flat)

kernel_name = 'gated_mix__spectral_fnet__mha_block__prompt'

def setup_inputs():

    _w = (lambda a: (lambda t: t.to(torch.bfloat16) if t.is_floating_point() else t)(torch.as_tensor(a)))
    def _A_setup():
        B, S, D = 1, 2048, 512
        ns = torch.arange(S)[:, None] * torch.arange(S)[None, :]
        nd = torch.arange(D)[:, None] * torch.arange(D)[None, :]
        Ws_re = torch.cos(-2.0 * torch.pi * ns / S).to(float)
        Ws_im = torch.sin(-2.0 * torch.pi * ns / S).to(float)
        Wd_re = torch.cos(-2.0 * torch.pi * nd / D).to(float)
        Wd_im = torch.sin(-2.0 * torch.pi * nd / D).to(float)
        return {
            "x": _w((lambda *s: torch.randn(*s, dtype=torch.bfloat16) * 0.05)(B, S, D)),
            "dft_seq_re": _w(Ws_re),
            "dft_seq_im": _w(Ws_im),
            "dft_hid_re": _w(Wd_re),
            "dft_hid_im": _w(Wd_im),
        }
    _A_in = _A_setup()

    def _B_setup():
        B, S, D, H = 1, 2048, 512, 32
        return {
            "x": _w((lambda *s: torch.randn(*s, dtype=torch.bfloat16) * 0.05)(B, S, D)),
            "wqkv": _w((lambda *s: torch.randn(*s, dtype=torch.bfloat16) * 1.6)(D, 3 * D)),
            "wo": _w((lambda *s: torch.randn(*s, dtype=torch.bfloat16) * 1.6)(D, D)),
        }
    _B_in = _B_setup()

    return {
        "A_x": _A_in["x"],
        "A_dft_seq_re": _A_in["dft_seq_re"],
        "A_dft_seq_im": _A_in["dft_seq_im"],
        "A_dft_hid_re": _A_in["dft_hid_re"],
        "A_dft_hid_im": _A_in["dft_hid_im"],
        "B_x": _B_in["x"],
        "B_wqkv": _B_in["wqkv"],
        "B_wo": _B_in["wo"],
        "gate_w": _w((lambda *s: torch.randn(*s, dtype=torch.bfloat16) * 0.05)(512) * 16.0),
    }

def reference(A_x, A_dft_seq_re, A_dft_seq_im, A_dft_hid_re, A_dft_hid_im, B_x, B_wqkv, B_wo, gate_w):
    def _A_ref(x, dft_seq_re, dft_seq_im, dft_hid_re, dft_hid_im):
        a = torch.einsum("bsd,ed->bse", x, dft_hid_re)
        b = torch.einsum("bsd,ed->bse", x, dft_hid_im)
        y = torch.einsum("ts,bse->bte", dft_seq_re, a) - torch.einsum("ts,bse->bte", dft_seq_im, b)
        return y

    def _B_ref(x, wqkv, wo):
        B, S, D = x.shape
        H = 32
        Dh = D // H
        qkv = (lambda x, *d: x.permute(*d))((x @ wqkv).reshape(B, S, 3, H, Dh), 2, 0, 3, 1, 4)
        q, k, v = qkv[0], qkv[1], qkv[2]
        scores = torch.einsum("bhqd,bhkd->bhqk", q, k) * (Dh ** -0.5)
        p = _softmax(scores, axis=-1)
        o = torch.einsum("bhqk,bhkd->bhqd", p, v)
        return (lambda x, *d: x.permute(*d))(o, 0, 2, 1, 3).reshape(B, S, D) @ wo

    x = A_x
    g = _sigmoid((x * gate_w).sum(-1, keepdims=True))
    a = _A_ref(x, A_dft_seq_re, A_dft_seq_im, A_dft_hid_re, A_dft_hid_im)
    b = _B_ref(x, B_wqkv, B_wo)
    return g * a + (1 - g) * b
```

```cpp
#include <hip/hip_runtime.h>
#include <hip/hip_bf16.h>
typedef __hip_bfloat16 bf16;
constexpr int S=2048, D=512, H=32, DH=16;
__device__ inline float b2f(bf16 v){ return __bfloat162float(v);}
__global__ void gemm(const bf16* A, int lda, const bf16* B, int ldb, int bt, bf16* C, int ldc, int M, int N, int K){
  __shared__ float As[16][17], Bs[16][17];
  int tx=threadIdx.x&15, ty=threadIdx.x>>4; int m=blockIdx.y*16+ty, n=blockIdx.x*16+tx; float acc=0;
  for(int k0=0;k0<K;k0+=16){
    As[ty][tx]=b2f(A[(long)m*lda+k0+tx]);
    if(bt) Bs[tx][ty]=b2f(B[(long)(blockIdx.x*16+ty)*ldb+k0+tx]); else Bs[ty][tx]=b2f(B[(long)(k0+ty)*ldb+n]);
    __syncthreads();
    for(int k=0;k<16;k++) acc+=As[ty][k]*Bs[k][tx];
    __syncthreads();
  }
  C[(long)m*ldc+n]=__float2bfloat16(acc);
}
__global__ void attn(const bf16* qkv, bf16* o){
  int h=blockIdx.y; int q=blockIdx.x*64+threadIdx.x;
  float qv[16]; for(int d=0;d<16;d++) qv[d]=b2f(qkv[(long)q*1536+h*16+d])*0.25f;
  float m=-1e30f,l=0,acc[16]; for(int d=0;d<16;d++) acc[d]=0;
  for(int k=0;k<S;k++){
    float s=0; for(int d=0;d<16;d++) s+=qv[d]*b2f(qkv[(long)k*1536+512+h*16+d]);
    float mn=fmaxf(m,s); float al=__expf(m-mn), p=__expf(s-mn); l=l*al+p;
    for(int d=0;d<16;d++) acc[d]=acc[d]*al+p*b2f(qkv[(long)k*1536+1024+h*16+d]);
    m=mn;
  }
  for(int d=0;d<16;d++) o[(long)q*512+h*16+d]=__float2bfloat16(acc[d]/l);
}
__global__ void fin(const bf16* x,const bf16* gw,const bf16* y1,const bf16* y2,const bf16* ob,bf16* out){
  int t=blockIdx.x; __shared__ float red[512];
  red[threadIdx.x]=b2f(x[t*512+threadIdx.x])*b2f(gw[threadIdx.x]); __syncthreads();
  for(int s=256;s>0;s>>=1){ if(threadIdx.x<s) red[threadIdx.x]+=red[threadIdx.x+s]; __syncthreads(); }
  float g=1.f/(1.f+__expf(-red[0]));
  int i=t*512+threadIdx.x;
  out[i]=__float2bfloat16(g*(b2f(y1[i])-b2f(y2[i]))+(1.f-g)*b2f(ob[i]));
}
extern "C" void kernel_launch(void* const* d_in, const int* in_sizes, int n_in, void* d_out, int out_size, void* d_ws, size_t ws_size, hipStream_t stream){
  const bf16* x=(const bf16*)d_in[0]; const bf16* cr=(const bf16*)d_in[1]; const bf16* ci=(const bf16*)d_in[2];
  const bf16* wr=(const bf16*)d_in[3]; const bf16* wi=(const bf16*)d_in[4]; const bf16* wqkv=(const bf16*)d_in[6]; const bf16* wo=(const bf16*)d_in[7]; const bf16* gw=(const bf16*)d_in[8];
  bf16* ws=(bf16*)d_ws; bf16* qkv=ws; bf16* a=qkv+S*1536; bf16* b=a+S*D; bf16* y1=b+S*D; bf16* y2=y1+S*D; bf16* o=y2+S*D; bf16* ob=o+S*D;
  gemm<<<dim3(1536/16,S/16),256,0,stream>>>(x,D,wqkv,1536,0,qkv,1536,S,1536,D);
  gemm<<<dim3(D/16,S/16),256,0,stream>>>(x,D,wr,D,1,a,D,S,D,D);
  gemm<<<dim3(D/16,S/16),256,0,stream>>>(x,D,wi,D,1,b,D,S,D,D);
  gemm<<<dim3(D/16,S/16),256,0,stream>>>(cr,S,a,D,0,y1,D,S,D,S);
  gemm<<<dim3(D/16,S/16),256,0,stream>>>(ci,S,b,D,0,y2,D,S,D,S);
  attn<<<dim3(S/64,H),64,0,stream>>>(qkv,o);
  gemm<<<dim3(D/16,S/16),256,0,stream>>>(o,D,wo,D,0,ob,D,S,D,D);
  fin<<<S,512,0,stream>>>(x,gw,y1,y2,ob,(bf16*)d_out);
}
```

```cpp
#include <hip/hip_runtime.h>
#include <hip/hip_bf16.h>
#include <stdint.h>

typedef float f32x16 __attribute__((ext_vector_type(16)));
typedef float f32x8 __attribute__((ext_vector_type(8)));
typedef float f32x4 __attribute__((ext_vector_type(4)));
typedef __bf16 bf16x8 __attribute__((ext_vector_type(8)));
typedef __bf16 bf16x16 __attribute__((ext_vector_type(16)));
typedef unsigned u32x4 __attribute__((ext_vector_type(4)));
typedef unsigned u32x2 __attribute__((ext_vector_type(2)));
typedef short s16x4 __attribute__((ext_vector_type(4)));
#define LAS __attribute__((address_space(3)))
typedef LAS char* lds_ptr;
#define DI __device__ __forceinline__
#define SB() __builtin_amdgcn_sched_barrier(0)
#define PIN(x) asm volatile("" : "+v"(x))
#define MFMA(a, b, c) __builtin_amdgcn_mfma_f32_32x32x16_bf16((a), (b), (c), 0, 0, 0)

constexpr int S = 2048, D = 512;
constexpr unsigned WS_Q = 0;
constexpr unsigned WS_K = 2u << 20;
constexpr unsigned WS_V = 4u << 20;
constexpr unsigned WS_AB = 6u << 20;
constexpr unsigned WS_O = 10u << 20;
constexpr unsigned WS_P = 12u << 20;
constexpr unsigned WS_G = 20u << 20;
constexpr unsigned WS_END = WS_G + 8192;
#define STAMP_BEGIN()
#define STAMP_END(kern)

DI u32x4 make_srd(const void* p, unsigned bytes) {
    const unsigned long long a = (unsigned long long)p;
    u32x4 r = {(unsigned)a, (unsigned)(a >> 32) & 0xffffu, bytes, 0x00020000u};
    return r;
}
DI void dma16(unsigned lds, unsigned voff, u32x4 srd, unsigned soff) {
    asm volatile("s_mov_b32 m0, %0\n\ts_nop 0\n\tbuffer_load_dwordx4 %1, %2, %3 offen lds" ::"s"(lds), "v"(voff), "s"(srd), "s"(soff) : "memory");
}
DI bf16x8 ldsfrag(lds_ptr p) { return *(const LAS bf16x8*)p; }
DI bf16x8 ldsfrag_tr(lds_ptr p0, lds_ptr p1) {
    const s16x4 lo = __builtin_amdgcn_ds_read_tr16_b64_v4i16((LAS s16x4*)p0);
    const s16x4 hi = __builtin_amdgcn_ds_read_tr16_b64_v4i16((LAS s16x4*)p1);
    return __builtin_bit_cast(bf16x8, __builtin_shufflevector(lo, hi, 0, 1, 2, 3, 4, 5, 6, 7));
}
DI unsigned pk2(float lo, float hi) {
    typedef __bf16 bf16x2 __attribute__((ext_vector_type(2)));
    typedef float f32x2 __attribute__((ext_vector_type(2)));
    const f32x2 v = {lo, hi};
    return __builtin_bit_cast(unsigned, __builtin_convertvector(v, bf16x2));
}
template <int B> DI u32x4 pack8(const f32x16& v, float sc) {
    u32x4 r = {pk2(v[B] * sc, v[B + 1] * sc), pk2(v[B + 2] * sc, v[B + 3] * sc), pk2(v[B + 4] * sc, v[B + 5] * sc), pk2(v[B + 6] * sc, v[B + 7] * sc)};
    return r;
}
DI u32x4 natural_order(u32x4 p) {
    const auto s0 = __builtin_amdgcn_permlane32_swap(p[0], p[2], false, false);
    const auto s1 = __builtin_amdgcn_permlane32_swap(p[1], p[3], false, false);
    u32x4 r = {s0[0], s1[0], s0[1], s1[1]};
    return r;
}
DI float bf_lo(unsigned u) { return __uint_as_float(u << 16); }
DI float bf_hi(unsigned u) { return __uint_as_float(u & 0xffff0000u); }
template <int B> DI f32x16 pv_mfma(bf16x8 vfrag, const f32x16& p, f32x16 o, int idx) {
    const f32x8 h = __builtin_shufflevector(p, p, B, B + 1, B + 2, B + 3, B + 4, B + 5, B + 6, B + 7);
    return __builtin_amdgcn_smfmac_f32_32x32x32_bf16(vfrag, __builtin_bit_cast(bf16x16, h), o, idx, 0, 0);
}
#define WAIT_BAR(N) asm volatile("s_waitcnt vmcnt(" #N ") lgkmcnt(0)\n\ts_barrier" ::: "memory")
template <int N> DI void wait_bar() { asm volatile("s_waitcnt vmcnt(%0) lgkmcnt(0)\n\ts_barrier" ::"i"(N) : "memory"); }

constexpr int P_X = 0, P_W = 65536, P_R = P_W + 4 * 12288, P_LDS = P_R + 4 * 8192;

template <int HALF>
DI void proj_body(const char* x, const char* wqkv, const char* wri, const char* gw, char* ws, lds_ptr lds, int mi, int nj, int w, int lane) {
    const int r = lane & 31, h = lane >> 5, wm = w & 3;
    const unsigned l0 = (unsigned)(uintptr_t)lds;
    const u32x4 sx = make_srd(x, S * D * 2), sw = make_srd(wqkv, D * 3 * D * 2), sr = make_srd(wri, D * D * 2);
    const int xrow = 8 * w + (lane >> 3), rrow = 8 * wm + (lane >> 3);
    const unsigned vx = xrow * 1024 + (((lane & 7) ^ ((xrow >> 1) & 7)) << 4);
    const unsigned vr = rrow * 1024 + (((lane & 7) ^ ((rrow >> 1) & 7)) << 4);
    const unsigned vw = (16 * wm + (lane >> 2)) * 3072 + (lane & 3) * 16;
    u32x4 gx, gv;
    {
        const char* px = x + (size_t)(mi * 128 + nj * 8 + w) * 1024 + lane * 16;
        const char* pg = gw + lane * 16;
        asm volatile("global_load_dwordx4 %0, %2, off\n\tglobal_load_dwordx4 %1, %3, off" : "=&v"(gx), "=&v"(gv) : "v"(px), "v"(pg) : "memory");
    }
    auto stage_dma = [&](int st) {
        const int sl = st & 3;
        dma16(l0 + P_X + sl * 16384 + w * 1024, vx, sx, mi * 131072 + st * 128);
        dma16(l0 + P_X + sl * 16384 + 8192 + w * 1024, vx, sx, mi * 131072 + st * 128 + 65536);
        if (HALF == 0) {
            dma16(l0 + P_W + sl * 12288 + wm * 1024, vw, sw, st * 64 * 3072 + nj * 64);
            dma16(l0 + P_W + sl * 12288 + 4096 + wm * 1024, vw, sw, st * 64 * 3072 + 1024 + nj * 64);
        } else {
            dma16(l0 + P_W + sl * 12288 + 8192 + wm * 1024, vw, sw, st * 64 * 3072 + 2048 + nj * 64);
        }
        dma16(l0 + P_R + sl * 8192 + w * 1024, vr, sr, nj * 32768 + st * 128);
    };
    constexpr int ND = HALF == 0 ? 5 : 4;
    stage_dma(0); stage_dma(1); stage_dma(2); stage_dma(3);

    lds_ptr xa[4], ra[4];
#pragma unroll
    for (int p = 0; p < 4; ++p) {
        const int sw16 = ((2 * p + h) ^ ((r >> 1) & 7)) << 4;
        xa[p] = lds + P_X + (32 * wm + r) * 128 + sw16;
        ra[p] = lds + P_R + r * 128 + sw16;
    }
    const lds_ptr wa = lds + P_W + (8 * h + ((lane & 15) >> 2)) * 64 + ((lane >> 4) & 1) * 32 + (lane & 3) * 8;
    f32x16 c0 = {}, c1 = {}, c2 = {};
    float gate = 0.f;
#pragma unroll
    for (int st = 0; st < 8; ++st) {
        const int sl = st & 3;
        if (st == 0) wait_bar<3 * ND>();
        else if (st <= 5) wait_bar<2 * ND>();
        else if (st == 6) wait_bar<ND>();
        else wait_bar<0>();
        if (st >= 1 && st + 3 < 8) stage_dma(st + 3);
        if (st == 0) {
            asm volatile("" : "+v"(gx), "+v"(gv));
#pragma unroll
            for (int i = 0; i < 4; ++i) gate += bf_lo(gx[i]) * bf_lo(gv[i]) + bf_hi(gx[i]) * bf_hi(gv[i]);
        }
#pragma unroll
        for (int p = 0; p < 4; ++p) {
            const bf16x8 xf = ldsfrag(xa[p] + sl * 16384);
            const lds_ptr wp = wa + sl * 12288 + p * 1024;
            if (HALF == 0) {
                c0 = MFMA(ldsfrag_tr(wp, wp + 256), xf, c0);
                c1 = MFMA(ldsfrag_tr(wp + 4096, wp + 4096 + 256), xf, c1);
            } else {
                c0 = MFMA(xf, ldsfrag_tr(wp + 8192, wp + 8192 + 256), c0);
                c1 = MFMA(xf, ldsfrag(ra[p] + sl * 8192), c1);
                c2 = MFMA(xf, ldsfrag(ra[p] + sl * 8192 + 4096), c2);
            }
        }
    }
    const int tile = mi * 4 + wm;
    if (HALF == 0) {
        constexpr float QS = 0.25f * 1.4426950408889634f;
        char* q = ws + WS_Q + (size_t)(2 * nj) * 65536 + tile * 1024 + lane * 16;
        *(u32x4*)q = pack8<0>(c0, QS);
        *(u32x4*)(q + 65536) = pack8<8>(c0, QS);
        const int slot = (r & 19) | ((r & 4) << 1) | ((r & 8) >> 1);
        char* k = ws + WS_K + (size_t)(2 * nj) * 65536 + tile * 1024 + h * 512 + slot * 16;
        *(u32x4*)k = pack8<0>(c1, 1.f);
        *(u32x4*)(k + 65536) = pack8<8>(c1, 1.f);
    } else {
        char* v = ws + WS_V + (size_t)(2 * nj + (r >> 4)) * 65536 + tile * 1024 + h * 256 + (r & 15) * 16;
        *(u32x4*)v = pack8<0>(c0, 1.f);
        *(u32x4*)(v + 512) = pack8<8>(c0, 1.f);
        char* a = ws + WS_AB + (size_t)nj * 262144 + tile * 2048 + lane * 16;
        *(u32x4*)a = natural_order(pack8<0>(c1, 1.f));
        *(u32x4*)(a + 1024) = natural_order(pack8<8>(c1, 1.f));
        *(u32x4*)(a + 131072) = natural_order(pack8<0>(c2, -1.f));
        *(u32x4*)(a + 131072 + 1024) = natural_order(pack8<8>(c2, -1.f));
    }
#pragma unroll
    for (int o = 32; o > 0; o >>= 1) gate += __shfl_xor(gate, o);
    if (lane == 0) ((float*)(ws + WS_G))[mi * 128 + nj * 8 + w] = 1.f / (1.f + __expf(-gate));
}

__global__ __launch_bounds__(512, 2) void k_proj(const char* x, const char* wqkv, const char* wr, const char* wi, const char* gw, char* ws) {
    extern __shared__ __attribute__((aligned(16))) char smem[];
    STAMP_BEGIN();
    const int lane = threadIdx.x & 63, w = __builtin_amdgcn_readfirstlane(threadIdx.x >> 6);
    const int g = blockIdx.x & 7, i = blockIdx.x >> 3;
    const int mi = (g & 1) * 8 + (i & 7), nj = (g >> 1) * 4 + (i >> 3);
    if (w < 4) proj_body<0>(x, wqkv, wr, gw, ws, (lds_ptr)smem, mi, nj, w, lane);
    else proj_body<1>(x, wqkv, wi, gw, ws, (lds_ptr)smem, mi, nj, w, lane);
    STAMP_END(0);
}

constexpr int M_C = 0, M_AB = 49152, M_KV = 98304, M_CONST = M_KV + 3 * 8192, M_LDS = M_CONST + 2048;
#define WITH_DFT 1

struct MidFrag { bf16x8 k, v0, v1, a0, a1, c0, c1; };

__global__ __launch_bounds__(512, 2) void k_mid(const char* cr, const char* ci, char* ws) {
    extern __shared__ __attribute__((aligned(16))) char smem[];
    STAMP_BEGIN();
    const lds_ptr lds = (lds_ptr)smem;
    const unsigned l0 = (unsigned)(uintptr_t)lds;
    const int lane = threadIdx.x & 63, w = __builtin_amdgcn_readfirstlane(threadIdx.x >> 6);
    const int r = lane & 31, h = lane >> 5, qg = w & 3, kh = w >> 2;
    const int g = blockIdx.x & 7, i = blockIdx.x >> 3;
    const int head = g * 4 + (i >> 3), qb = i & 7;
    const int sp = g >> 1, mt = (g & 1) * 8 + (i >> 2), nt = i & 3;

    u32x4 sc = make_srd(sp < 2 ? cr : ci, S * S * 2), sw = make_srd(ws, WS_END);
    const int crow = 8 * w + (lane >> 3);
    const unsigned vc = crow * 4096 + (((lane & 7) ^ ((crow >> 1) & 7)) << 4), vl = lane * 16;
    unsigned oc = mt * 128 * 4096 + (sp & 1) * 2048;
    unsigned oab = WS_AB + (nt * 4 + (w >> 1)) * 262144 + (sp * 64 + (w & 1) * 2) * 1024;
    unsigned okv = ((w & 2) ? WS_V : WS_K) + head * 65536 + (kh * 32 + (w & 1)) * 1024;
    const unsigned dc = l0 + M_C + w * 1024, dab = l0 + M_AB + w * 2048, dkv = l0 + M_KV + w * 1024;
    bf16x8 q0, q1;
    {
        const char* pq = ws + WS_Q + (size_t)head * 65536 + (qb * 8 + qg * 2) * 1024 + lane * 16;
        asm volatile("global_load_dwordx4 %0, %2, off\n\tglobal_load_dwordx4 %1, %2, off offset:1024" : "=&v"(q0), "=&v"(q1) : "v"(pq) : "memory");
    }
#define DMA_P(P, SL) do { \
        if ((P) == 0) dma16(dkv + (SL) * 8192, vl, sw, okv); \
        else if ((P) == 1) { if (WITH_DFT) dma16(dc + (SL) * 16384, vc, sc, oc); } \
        else if ((P) == 2) { if (WITH_DFT) dma16(dc + (SL) * 16384 + 8192, vc, sc, oc + 262144); } \
        else if ((P) == 3) { if (WITH_DFT) dma16(dab + (SL) * 16384, vl, sw, oab); } \
        else { if (WITH_DFT) dma16(dab + (SL) * 16384 + 1024, vl, sw, oab + 1024); okv += 2048; oc += 128; oab += 4096; } } while (0)
    constexpr int ND = WITH_DFT ? 5 : 1;
#pragma unroll
    for (int st = 0; st < 3; ++st) { DMA_P(0, st); DMA_P(1, st); DMA_P(2, st); DMA_P(3, st); DMA_P(4, st); }

    if (threadIdx.x < 128) {
        const unsigned one2 = ((threadIdx.x & 11) == 0) ? 0x3f803f80u : 0u;
        const u32x4 cv = {one2, one2, one2, one2};
        *(LAS u32x4*)(lds + M_CONST + threadIdx.x * 16) = cv;
    }
    const int cslot = (r == 16) ? 0 : (r == 20) ? 4 : (r < 20 || r >= 28) ? 1 : 5;
    lds_ptr va[3];
#pragma unroll
    for (int s = 0; s < 3; ++s) va[s] = (r < 16) ? lds + M_KV + s * 8192 + kh * 4096 + 2048 + h * 256 + r * 16 : lds + M_CONST + cslot * 16;
    const lds_ptr ka = lds + M_KV + kh * 4096 + lane * 16;
    const lds_ptr aa = lds + M_AB + (qg >> 1) * 8192 + kh * 1024 + lane * 16;
    lds_ptr ca[2];
#pragma unroll
    for (int u = 0; u < 2; ++u) ca[u] = lds + M_C + ((qg & 1) * 64 + r) * 128 + (((2 * (2 * u + kh) + h) ^ ((r >> 1) & 7)) << 4);

#define RD_K(F, SL, U) F.k = ldsfrag(ka + (SL) * 8192 + (U) * 1024)
#define RD_V0(F, SL, U) F.v0 = ldsfrag(va[SL] + (U) * 1024)
#define RD_V1(F, SL, U) F.v1 = ldsfrag(va[SL] + (U) * 1024 + 512)
#define RD_A0(F, SL, U) if (WITH_DFT) F.a0 = ldsfrag(aa + (SL) * 16384 + (U) * 2048)
#define RD_A1(F, SL, U) if (WITH_DFT) F.a1 = ldsfrag(aa + (SL) * 16384 + (U) * 2048 + 4096)
#define RD_C0(F, SL, U) if (WITH_DFT) F.c0 = ldsfrag(ca[U] + (SL) * 16384)
#define RD_C1(F, SL, U) if (WITH_DFT) F.c1 = ldsfrag(ca[U] + (SL) * 16384 + 4096)

    int idx = (int)0xDDDDDDDD;
    PIN(idx);
    f32x16 o0 = {}, o1 = {}, y00 = {}, y01 = {}, y10 = {}, y11 = {}, s0, s1 = {};
    const f32x16 zero = {};
    MidFrag fa, fb;
    wait_bar<2 * ND>();
    asm volatile("" : "+v"(q0), "+v"(q1));
    RD_K(fa, 0, 0); RD_V0(fa, 0, 0); RD_V1(fa, 0, 0); RD_A0(fa, 0, 0); RD_A1(fa, 0, 0); RD_C0(fa, 0, 0); RD_C1(fa, 0, 0);
    fb.v1 = fa.v1;
    s0 = MFMA(fa.k, q0, zero);

#define EX3(X, i) do { X[i] = __builtin_amdgcn_exp2f(X[i]); X[i + 1] = __builtin_amdgcn_exp2f(X[i + 1]); X[i + 2] = __builtin_amdgcn_exp2f(X[i + 2]); PIN(X); SB(); } while (0)
#define EX4(X, i) do { X[i] = __builtin_amdgcn_exp2f(X[i]); X[i + 1] = __builtin_amdgcn_exp2f(X[i + 1]); X[i + 2] = __builtin_amdgcn_exp2f(X[i + 2]); X[i + 3] = __builtin_amdgcn_exp2f(X[i + 3]); PIN(X); SB(); } while (0)
#define DFT(Y, A, C) do { if (WITH_DFT) { Y = MFMA(A, C, Y); PIN(Y); } SB(); } while (0)
#define HALFSTAGE(F, P, NSL, NU, RD, DM, DSL) do { SB(); \
        DFT(y00, F.a0, F.c0);                                   EX3(s0, 0); \
        o1 = pv_mfma<8>(P.v1, s1, o1, idx); PIN(o1); SB(); \
        if (RD) { RD_K(P, NSL, NU); RD_V0(P, NSL, NU); SB(); }  EX3(s0, 3); \
        s1 = MFMA(F.k, q1, zero); PIN(s1); SB(); \
        if (DM) { DMA_P(0, DSL); SB(); }                        EX3(s0, 6); \
        DFT(y01, F.a0, F.c1); \
        if (RD) { RD_V1(P, NSL, NU); RD_A0(P, NSL, NU); SB(); } EX3(s0, 9); \
        o0 = pv_mfma<0>(F.v0, s0, o0, idx); PIN(o0); SB(); \
        if (DM) { DMA_P(1, DSL); SB(); }                        EX4(s0, 12); \
        DFT(y10, F.a1, F.c0); \
        if (DM) { DMA_P(2, DSL); SB(); }                        EX3(s1, 0); \
        o0 = pv_mfma<8>(F.v1, s0, o0, idx); PIN(o0); SB(); \
        if (RD) { RD_C0(P, NSL, NU); RD_A1(P, NSL, NU); SB(); } EX3(s1, 3); \
        if (RD) { s0 = MFMA(P.k, q0, zero); PIN(s0); SB(); } \
        if (DM) { DMA_P(3, DSL); SB(); }                        EX3(s1, 6); \
        DFT(y11, F.a1, F.c1); \
        if (RD) { RD_C1(P, NSL, NU); SB(); }                    EX3(s1, 9); \
        o1 = pv_mfma<0>(F.v0, s1, o1, idx); PIN(o1); SB(); \
        if (DM) { DMA_P(4, DSL); SB(); }                        EX4(s1, 12); \
    } while (0)
#define STAGE(SL) do { \
        HALFSTAGE(fa, fb, SL, 1, true, false, 0); \
        wait_bar<ND>(); \
        HALFSTAGE(fb, fa, ((SL) + 1) % 3, 0, true, true, SL); } while (0)

    for (int it = 0; it < 5; ++it) {
        STAGE(0);
        if (it == 4) { sc[2] = 0; sw[2] = 0; }
        STAGE(1);
        STAGE(2);
    }
    HALFSTAGE(fa, fb, 0, 1, true, false, 0);
    HALFSTAGE(fb, fa, 0, 0, false, false, 0);
    o1 = pv_mfma<8>(fb.v1, s1, o1, idx);
    wait_bar<0>();

    {
        const lds_ptr mine = lds + w * 12288 + lane * 16, other = lds + (w ^ 4) * 12288 + lane * 16;
        const f32x16 os = kh ? o0 : o1;
        const f32x16 ys0 = kh ? y00 : y01, ys1 = kh ? y10 : y11;
#pragma unroll
        for (int j = 0; j < 3; ++j) *(LAS f32x4*)(mine + j * 1024) = f32x4{os[4 * j], os[4 * j + 1], os[4 * j + 2], os[4 * j + 3]};
        if (WITH_DFT) {
#pragma unroll
            for (int j = 0; j < 4; ++j) {
                *(LAS f32x4*)(mine + (3 + j) * 1024) = f32x4{ys0[4 * j], ys0[4 * j + 1], ys0[4 * j + 2], ys0[4 * j + 3]};
                *(LAS f32x4*)(mine + (7 + j) * 1024) = f32x4{ys1[4 * j], ys1[4 * j + 1], ys1[4 * j + 2], ys1[4 * j + 3]};
            }
        }
        wait_bar<0>();
        f32x16 o = kh ? o1 : o0, ya = kh ? y01 : y00, yb = kh ? y11 : y10;
#pragma unroll
        for (int j = 0; j < 3; ++j) {
            const f32x4 t = *(const LAS f32x4*)(other + j * 1024);
#pragma unroll
            for (int e = 0; e < 4; ++e) o[4 * j + e] += t[e];
        }
        if (WITH_DFT) {
#pragma unroll
            for (int j = 0; j < 4; ++j) {
                const f32x4 t0 = *(const LAS f32x4*)(other + (3 + j) * 1024), t1 = *(const LAS f32x4*)(other + (7 + j) * 1024);
#pragma unroll
                for (int e = 0; e < 4; ++e) { ya[4 * j + e] += t0[e]; yb[4 * j + e] += t1[e]; }
            }
            char* p = ws + WS_P + (size_t)sp * (2u << 20) + (size_t)(mt * 4 + (qg & 1) * 2 + kh) * 32768 + (nt * 4 + (qg >> 1) * 2) * 2048 + lane * 16;
            *(u32x4*)p = pack8<0>(ya, 1.f);
            *(u32x4*)(p + 1024) = pack8<8>(ya, 1.f);
            *(u32x4*)(p + 2048) = pack8<0>(yb, 1.f);
            *(u32x4*)(p + 3072) = pack8<8>(yb, 1.f);
        }
        bool ok = o[8] >= 8.271806e-25f && o[8] <= 1.2676506e30f;
#pragma unroll
        for (int e = 0; e < 8; ++e) ok = ok && (__builtin_fabsf(o[e]) <= 3.0e38f);
        if (__builtin_expect(__any(!ok), 0)) {
            const bf16x8 q = kh ? q1 : q0;
            const char* kp = ws + WS_K + (size_t)head * 65536 + lane * 16;
            const char* vp = ws + WS_V + (size_t)head * 65536 + h * 256 + (r & 15) * 16;
            float m = -__builtin_inff();
            for (int t = 0; t < 64; ++t) {
                const f32x16 s = MFMA(*(const bf16x8*)(kp + t * 1024), q, zero);
#pragma unroll
                for (int e = 0; e < 16; ++e) m = __builtin_fmaxf(m, s[e]);
            }
            m = __builtin_fmaxf(m, __shfl_xor(m, 32));
            const unsigned cw = (r == 16 || r == 20) ? 0x3f803f80u : 0u;
            const u32x4 cv = {cw, cw, cw, cw};
            o = zero;
            for (int t = 0; t < 64; ++t) {
                f32x16 s = MFMA(*(const bf16x8*)(kp + t * 1024), q, zero);
                const u32x4 v0 = *(const u32x4*)(vp + t * 1024), v1 = *(const u32x4*)(vp + t * 1024 + 512);
#pragma unroll
                for (int e = 0; e < 16; ++e) s[e] = __builtin_amdgcn_exp2f(s[e] - m);
                o = pv_mfma<0>(__builtin_bit_cast(bf16x8, r < 16 ? v0 : cv), s, o, idx);
                o = pv_mfma<8>(__builtin_bit_cast(bf16x8, r < 16 ? v1 : cv), s, o, idx);
            }
        }
        *(u32x4*)(ws + WS_O + (size_t)(qb * 8 + qg * 2 + kh) * 32768 + head * 1024 + lane * 16) = pack8<0>(o, 1.f / o[8]);
    }
    STAMP_END(1);
}

constexpr int O_O = 0, O_W = 65536, O_LDS = 131072, O_PITCH = 272;

__global__ __launch_bounds__(512, 2) void k_out(const char* wo, char* ws, char* out) {
    extern __shared__ __attribute__((aligned(16))) char smem[];
    STAMP_BEGIN();
    const lds_ptr lds = (lds_ptr)smem;
    const unsigned l0 = (unsigned)(uintptr_t)lds;
    const int lane = threadIdx.x & 63, w = __builtin_amdgcn_readfirstlane(threadIdx.x >> 6);
    const int r = lane & 31, h = lane >> 5, tb = (w >> 1) & 1, eb = w & 1, kh = w >> 2;
    const int g = blockIdx.x & 7, i = blockIdx.x >> 3;
    const int tt = g * 4 + (i >> 3), et = i & 7;
    const u32x4 so = make_srd(wo, D * D * 2), sw = make_srd(ws, WS_END);
    u32x4 p0, p1, p2, p3;
    float gate;
    {
        const char* pp = ws + WS_P + (size_t)(2 * kh) * (2u << 20) + (size_t)(tt * 2 + tb) * 32768 + (et * 2 + eb) * 2048 + lane * 16;
        const char* pq = pp + (2u << 20);
        const char* pg = ws + WS_G + (tt * 64 + tb * 32 + r) * 4;
        asm volatile("global_load_dwordx4 %0, %5, off\n\tglobal_load_dwordx4 %1, %5, off offset:1024\n\t"
                     "global_load_dwordx4 %2, %6, off\n\tglobal_load_dwordx4 %3, %6, off offset:1024\n\tglobal_load_dword %4, %7, off"
                     : "=&v"(p0), "=&v"(p1), "=&v"(p2), "=&v"(p3), "=&v"(gate) : "v"(pp), "v"(pq), "v"(pg) : "memory");
    }
    const int wrow = lane >> 3;
    const unsigned vw = wrow * 1024 + (((lane & 7) ^ ((wrow & 2) << 1)) << 4), vl = lane * 16;
#pragma unroll
    for (int c = 0; c < 4; ++c) {
        const int ks = kh * 16 + c * 4 + (w & 3);
        dma16(l0 + O_O + ks * 1024, vl, sw, WS_O + (tt * 2) * 32768 + ks * 1024);
        dma16(l0 + O_O + 32768 + ks * 1024, vl, sw, WS_O + (tt * 2 + 1) * 32768 + ks * 1024);
        dma16(l0 + O_W + ks * 2048, vw, so, ks * 16384 + et * 128);
        dma16(l0 + O_W + ks * 2048 + 1024, vw, so, ks * 16384 + 8192 + et * 128);
    }
    const int q = (lane & 15) >> 2;
    const lds_ptr oa = lds + O_O + tb * 32768 + kh * 16384 + lane * 16;
    const lds_ptr wa = lds + O_W + kh * 32768 + (4 * h + q) * 128 + ((eb * 64) ^ ((q & 2) << 5)) + ((lane >> 4) & 1) * 32 + (lane & 3) * 8;
    f32x16 acc = {};
#pragma unroll
    for (int c = 0; c < 4; ++c) {
        if (c == 0) wait_bar<12>(); else if (c == 1) wait_bar<8>(); else if (c == 2) wait_bar<4>(); else wait_bar<0>();
#pragma unroll
        for (int p = 0; p < 4; ++p) {
            const int ks = c * 4 + p;
            acc = MFMA(ldsfrag_tr(wa + ks * 2048, wa + ks * 2048 + 1024), ldsfrag(oa + ks * 1024), acc);
        }
    }
    asm volatile("" : "+v"(p0), "+v"(p1), "+v"(p2), "+v"(p3), "+v"(gate));
    wait_bar<0>();
    {
        const float og = 1.f - gate;
        const lds_ptr st = lds + kh * (64 * O_PITCH) + (tb * 32 + r) * O_PITCH + (eb * 32 + 4 * h) * 4;
#pragma unroll
        for (int j = 0; j < 4; ++j) {
            const u32x4 a = j < 2 ? p0 : p1, b = j < 2 ? p2 : p3;
            const unsigned a0 = a[(j & 1) * 2], a1 = a[(j & 1) * 2 + 1], b0 = b[(j & 1) * 2], b1 = b[(j & 1) * 2 + 1];
            const f32x4 v = {gate * (bf_lo(a0) + bf_lo(b0)) + og * acc[4 * j], gate * (bf_hi(a0) + bf_hi(b0)) + og * acc[4 * j + 1],
                             gate * (bf_lo(a1) + bf_lo(b1)) + og * acc[4 * j + 2], gate * (bf_hi(a1) + bf_hi(b1)) + og * acc[4 * j + 3]};
            *(LAS f32x4*)(st + j * 32) = v;
        }
    }
    wait_bar<0>();
    {
        const int row = threadIdx.x >> 3, ch = threadIdx.x & 7;
        const lds_ptr rd = lds + row * O_PITCH + ch * 32;
        const f32x4 a0 = *(const LAS f32x4*)rd, a1 = *(const LAS f32x4*)(rd + 16);
        const f32x4 b0 = *(const LAS f32x4*)(rd + 64 * O_PITCH), b1 = *(const LAS f32x4*)(rd + 64 * O_PITCH + 16);
        const u32x4 o = {pk2(a0[0] + b0[0], a0[1] + b0[1]), pk2(a0[2] + b0[2], a0[3] + b0[3]), pk2(a1[0] + b1[0], a1[1] + b1[1]), pk2(a1[2] + b1[2], a1[3] + b1[3])};
        *(u32x4*)(out + (size_t)(tt * 64 + row) * 1024 + et * 128 + ch * 16) = o;
    }
    STAMP_END(2);
}

extern "C" void kernel_launch(void* const* d_in, const int* in_sizes, int n_in, void* d_out, int out_size, void* d_ws, size_t ws_size, hipStream_t stream) {
    (void)in_sizes; (void)n_in; (void)out_size; (void)ws_size;
    (void)hipFuncSetAttribute((const void*)k_proj, hipFuncAttributeMaxDynamicSharedMemorySize, P_LDS);
    (void)hipFuncSetAttribute((const void*)k_mid, hipFuncAttributeMaxDynamicSharedMemorySize, M_LDS);
    (void)hipFuncSetAttribute((const void*)k_out, hipFuncAttributeMaxDynamicSharedMemorySize, O_LDS);
    const char* x = (const char*)d_in[0];
    char* ws = (char*)d_ws;
    k_proj<<<256, 512, P_LDS, stream>>>(x, (const char*)d_in[6], (const char*)d_in[3], (const char*)d_in[4], (const char*)d_in[8], ws);
    k_mid<<<256, 512, M_LDS, stream>>>((const char*)d_in[1], (const char*)d_in[2], ws);
    k_out<<<256, 512, O_LDS, stream>>>((const char*)d_in[7], ws, (char*)d_out);
}
```

```cpp
#include <hip/hip_runtime.h>
#include <hip/hip_bf16.h>
#include <stdint.h>

typedef unsigned long long u64;
typedef float f32x16 __attribute__((ext_vector_type(16)));
typedef float f32x8 __attribute__((ext_vector_type(8)));
typedef float f32x4 __attribute__((ext_vector_type(4)));
typedef __bf16 bf16x8 __attribute__((ext_vector_type(8)));
typedef __bf16 bf16x16 __attribute__((ext_vector_type(16)));
typedef unsigned u32x4 __attribute__((ext_vector_type(4)));
typedef short s16x4 __attribute__((ext_vector_type(4)));
#define LAS __attribute__((address_space(3)))
typedef LAS char* lds_ptr;
#define DI __device__ __forceinline__
#define SB() __builtin_amdgcn_sched_barrier(0)
#define PIN(x) asm volatile("" : "+v"(x))
#define MFMA(a, b, c) __builtin_amdgcn_mfma_f32_32x32x16_bf16((a), (b), (c), 0, 0, 0)

constexpr int S = 2048, D = 512;
#define LEAD 7
constexpr unsigned WS_Q = 0;
constexpr unsigned WS_K = 2u << 20;
constexpr unsigned WS_V = 4u << 20;
constexpr unsigned WS_AB = 6u << 20;
constexpr unsigned WS_O = 10u << 20;
constexpr unsigned WS_P = 12u << 20;
constexpr unsigned WS_G = 20u << 20;
constexpr unsigned WS_F = WS_G + 8192;
constexpr unsigned WS_GEN = WS_F + 8192;
constexpr unsigned WS_END = WS_GEN + 256;
constexpr unsigned SPIN_CAP = 1u << 20;

#define STAMP_BEGIN()
#define STAMP(i)
#define STAMP_END()

DI u32x4 make_srd(const void* p, unsigned bytes) {
    const u64 a = (u64)p;
    u32x4 r = {(unsigned)a, (unsigned)(a >> 32) & 0xffffu, bytes, 0x00020000u};
    return r;
}
DI void dma16(unsigned lds, unsigned voff, u32x4 srd, unsigned soff) {
    asm volatile("s_mov_b32 m0, %0\n\ts_nop 0\n\tbuffer_load_dwordx4 %1, %2, %3 offen lds" ::"s"(lds), "v"(voff), "s"(srd), "s"(soff) : "memory");
}
DI void dma16c(unsigned lds, unsigned voff, u32x4 srd, unsigned soff) {
    asm volatile("s_mov_b32 m0, %0\n\ts_nop 0\n\tbuffer_load_dwordx4 %1, %2, %3 offen sc1 lds" ::"s"(lds), "v"(voff), "s"(srd), "s"(soff) : "memory");
}
DI void store16c(u32x4 v, u32x4 srd, unsigned voff, unsigned soff) {
    asm volatile("buffer_store_dwordx4 %0, %1, %2, %3 offen sc1\n\ts_nop 1" ::"v"(v), "v"(voff), "s"(srd), "s"(soff) : "memory");
}
DI void post_flag(char* ws, unsigned off, u64 epoch) { __hip_atomic_store((u64*)(ws + off), epoch, __ATOMIC_RELAXED, __HIP_MEMORY_SCOPE_AGENT); }
DI void poll_issue(u64& v, const char* p) { asm volatile("global_load_dwordx2 %0, %1, off sc1" : "=v"(v) : "v"(p) : "memory"); }
DI void poll_finish(u64 v, const char* p, bool active, u64 epoch) {
    asm volatile("" : "+v"(v));
    unsigned spins = 0;
    while (!__all(!active || v == epoch)) {
        if (++spins > SPIN_CAP) break;
        __builtin_amdgcn_s_sleep(1);
        asm volatile("global_load_dwordx2 %0, %1, off sc1\n\ts_waitcnt vmcnt(0)" : "=&v"(v) : "v"(p) : "memory");
    }
}
DI bf16x8 ldsfrag(lds_ptr p) { return *(const LAS bf16x8*)p; }
DI bf16x8 ldsfrag_tr(lds_ptr p0, lds_ptr p1) {
    const s16x4 lo = __builtin_amdgcn_ds_read_tr16_b64_v4i16((LAS s16x4*)p0);
    const s16x4 hi = __builtin_amdgcn_ds_read_tr16_b64_v4i16((LAS s16x4*)p1);
    return __builtin_bit_cast(bf16x8, __builtin_shufflevector(lo, hi, 0, 1, 2, 3, 4, 5, 6, 7));
}
DI unsigned pk2(float lo, float hi) {
    typedef __bf16 bf16x2 __attribute__((ext_vector_type(2)));
    typedef float f32x2 __attribute__((ext_vector_type(2)));
    const f32x2 v = {lo, hi};
    return __builtin_bit_cast(unsigned, __builtin_convertvector(v, bf16x2));
}
template <int B> DI u32x4 pack8(const f32x16& v, float sc) {
    u32x4 r = {pk2(v[B] * sc, v[B + 1] * sc), pk2(v[B + 2] * sc, v[B + 3] * sc), pk2(v[B + 4] * sc, v[B + 5] * sc), pk2(v[B + 6] * sc, v[B + 7] * sc)};
    return r;
}
DI u32x4 natural_order(u32x4 p) {
    const auto s0 = __builtin_amdgcn_permlane32_swap(p[0], p[2], false, false);
    const auto s1 = __builtin_amdgcn_permlane32_swap(p[1], p[3], false, false);
    u32x4 r = {s0[0], s1[0], s0[1], s1[1]};
    return r;
}
DI float bf_lo(unsigned u) { return __uint_as_float(u << 16); }
DI float bf_hi(unsigned u) { return __uint_as_float(u & 0xffff0000u); }
template <int B> DI f32x16 pv_mfma(bf16x8 vfrag, const f32x16& p, f32x16 o, int idx) {
    const f32x8 h = __builtin_shufflevector(p, p, B, B + 1, B + 2, B + 3, B + 4, B + 5, B + 6, B + 7);
    return __builtin_amdgcn_smfmac_f32_32x32x32_bf16(vfrag, __builtin_bit_cast(bf16x16, h), o, idx, 0, 0);
}
template <int N> DI void wait_vm() { asm volatile("s_waitcnt vmcnt(%0) lgkmcnt(0)" ::"i"(N) : "memory"); }
DI void bar() { asm volatile("s_barrier" ::: "memory"); }
template <int N> DI void wait_bar() { wait_vm<N>(); bar(); }

constexpr int P_SLOT = 24576, P_W0 = 98304, P_W1 = 49152;
constexpr int M_C = 0, M_AB = 49152, M_KV = 98304, M_CONST = M_KV + 3 * 8192, M_X = 131072;
constexpr int LDS_BYTES = 163840, O_PITCH = 272;
constexpr int wo_chunk(int c) { return c == 0 ? 16384 : c == 1 ? 65536 : c == 2 ? 32768 : 81920; }
constexpr int o_chunk(int c) { return c == 0 ? 0 : c == 1 ? 49152 : c == 2 ? 98304 : 114688; }
constexpr int KV_AT = LEAD - 2, LAST = LEAD + 15;
constexpr int ops_after(int j) {
    int n = 0;
    if (j <= 14) n += 4;
    if (j == KV_AT) n += 5;
    if (j >= LEAD && j + 3 - LEAD < 16) n += 1;
    if (j == 15) n += 4;
    if (j == LAST - 2) n += 5;
    return n;
}
static_assert(LEAD >= 5 && LEAD <= 12, "phase 2 schedule");

struct Frag { bf16x8 k, v0, v1, a0, a1, c0, a2, a3, c1; };

struct Mid {
    lds_ptr lds, ka, va[3], aa, ca[4];
    unsigned l0, vc, vl, vwo, oc, oab, okv, owo, w;
    u32x4 sc, sw, so;
    __amdgpu_buffer_rsrc_t rw;
    char* ws;
    const char *pq, *pollb, *pollc, *pp, *pg;
    unsigned op, fo2, fo4;
    u64 epoch, pv;
    int lane, idx;
    bf16x8 q0, q1;
    f32x16 o0, o1, y0, y1, s0, s1;
    Frag fa, fb;
    u32x4 p0, p1, p2, p3;
    float gate;
};

template <int J, int P> DI void mid_dma(Mid& m) {
    constexpr int SL = J % 3;
    if constexpr (J + 3 < 16) {
        if constexpr (P == 1) dma16(m.l0 + M_C + SL * 16384 + m.w * 1024, m.vc, m.sc, m.oc + (J + 3) * 128);
        if constexpr (P == 2) dma16(m.l0 + M_C + SL * 16384 + 8192 + m.w * 1024, m.vc, m.sc, m.oc + (J + 3) * 128 + 262144);
        if constexpr (P == 3) dma16c(m.l0 + M_AB + SL * 16384 + m.w * 2048, m.vl, m.sw, m.oab + (J + 3) * 4096);
        if constexpr (P == 4) dma16c(m.l0 + M_AB + SL * 16384 + m.w * 2048 + 1024, m.vl, m.sw, m.oab + (J + 3) * 4096 + 1024);
    } else if constexpr (J <= 14 && P >= 1) {
        constexpr int c = (J - 13) * 2 + (P - 1) / 2, hf = (P - 1) & 1;
        dma16(m.l0 + wo_chunk(c) + m.w * 2048 + hf * 1024, m.vwo, m.so, m.owo + c * 131072 + hf * 8192);
    }
    if constexpr (P == 0) {
        if constexpr (J == KV_AT) {
            asm volatile("global_load_dwordx4 %0, %2, off sc1\n\tglobal_load_dwordx4 %1, %3, off sc1" : "=&v"(m.q0), "=&v"(m.q1) : "v"(m.pq), "v"(m.pq + (m.w < 4 ? 1024 : -1024)) : "memory");
#pragma unroll
            for (int s = 0; s < 3; ++s) dma16c(m.l0 + M_KV + s * 8192 + m.w * 1024, m.vl, m.sw, m.okv + s * 2048);
        }
        if constexpr (J >= LEAD && J + 3 - LEAD < 16) dma16c(m.l0 + M_KV + ((J - LEAD) % 3) * 8192 + m.w * 1024, m.vl, m.sw, m.okv + (J + 3 - LEAD) * 2048);
        if constexpr (J == LAST - 2)
            asm volatile("global_load_dwordx4 %0, %5, off sc1\n\tglobal_load_dwordx4 %1, %5, off offset:1024 sc1\n\t"
                         "global_load_dwordx4 %2, %6, off sc1\n\tglobal_load_dwordx4 %3, %6, off offset:1024 sc1\n\tglobal_load_dword %4, %7, off sc1"
                         : "=&v"(m.p0), "=&v"(m.p1), "=&v"(m.p2), "=&v"(m.p3), "=&v"(m.gate) : "v"(m.pp), "v"(m.pp + (2u << 20)), "v"(m.pg) : "memory");
    }
}

#define EX3(X, i) do { if constexpr (ATT) { X[i] = __builtin_amdgcn_exp2f(X[i]); X[i + 1] = __builtin_amdgcn_exp2f(X[i + 1]); X[i + 2] = __builtin_amdgcn_exp2f(X[i + 2]); PIN(X); SB(); } } while (0)
#define EX4(X, i) do { if constexpr (ATT) { X[i] = __builtin_amdgcn_exp2f(X[i]); X[i + 1] = __builtin_amdgcn_exp2f(X[i + 1]); X[i + 2] = __builtin_amdgcn_exp2f(X[i + 2]); X[i + 3] = __builtin_amdgcn_exp2f(X[i + 3]); PIN(X); SB(); } } while (0)
#define DFTM(Y, A, C) do { if constexpr (DFT) { Y = MFMA(A, C, Y); PIN(Y); SB(); } } while (0)

template <int J, int U> DI void half_stage(Mid& m, Frag& F, Frag& N) {
    constexpr int NJ = U ? J + 1 : J, NU = U ^ 1;
    constexpr bool DFT = J < 16, ATT = J >= LEAD, NDFT = NJ < 16, NATT = NJ >= LEAD && NJ <= LAST, FIRST = J == LEAD && U == 0;
    constexpr int CS = (NJ % 3) * 16384, KS = NATT ? (NJ - LEAD) % 3 : 0;
    const f32x16 zero = {};
    SB();
    DFTM(m.y0, F.a0, F.c0);
    if constexpr (NATT) { N.k = ldsfrag(m.ka + KS * 8192 + NU * 1024); SB(); }
    EX3(m.s0, 0);
    if constexpr (ATT && !FIRST) { m.o1 = pv_mfma<8>(N.v1, m.s1, m.o1, m.idx); PIN(m.o1); SB(); }
    if constexpr (NDFT) { N.a0 = ldsfrag(m.aa + CS + NU * 2048); N.c0 = ldsfrag(m.ca[2 * NU] + CS); SB(); }
    EX3(m.s0, 3);
    if constexpr (ATT) { m.s1 = MFMA(F.k, m.q1, zero); PIN(m.s1); SB(); }
    if constexpr (U) { mid_dma<J, 0>(m); SB(); }
    EX3(m.s0, 6);
    DFTM(m.y1, F.a1, F.c0);
    if constexpr (NATT) { N.v0 = ldsfrag(m.va[KS] + NU * 1024); N.v1 = ldsfrag(m.va[KS] + NU * 1024 + 512); SB(); }
    EX3(m.s0, 9);
    if constexpr (ATT) { m.o0 = pv_mfma<0>(F.v0, m.s0, m.o0, m.idx); PIN(m.o0); SB(); }
    if constexpr (U) { mid_dma<J, 1>(m); SB(); }
    EX4(m.s0, 12);
    DFTM(m.y0, F.a2, F.c1);
    if constexpr (NDFT) { N.a1 = ldsfrag(m.aa + CS + NU * 2048 + 4096); SB(); }
    if constexpr (U) { mid_dma<J, 2>(m); SB(); }
    EX3(m.s1, 0);
    if constexpr (ATT) { m.o0 = pv_mfma<8>(F.v1, m.s0, m.o0, m.idx); PIN(m.o0); SB(); }
    if constexpr (NDFT) { N.a2 = ldsfrag(m.aa + CS + NU * 2048 + 1024); N.c1 = ldsfrag(m.ca[2 * NU + 1] + CS); SB(); }
    EX3(m.s1, 3);
    if constexpr (NATT) { m.s0 = MFMA(N.k, m.q0, zero); PIN(m.s0); SB(); }
    if constexpr (U) { mid_dma<J, 3>(m); SB(); }
    EX3(m.s1, 6);
    DFTM(m.y1, F.a3, F.c1);
    if constexpr (NDFT) { N.a3 = ldsfrag(m.aa + CS + NU * 2048 + 4096 + 1024); SB(); }
    EX3(m.s1, 9);
    if constexpr (ATT) { m.o1 = pv_mfma<0>(F.v0, m.s1, m.o1, m.idx); PIN(m.o1); SB(); }
    if constexpr (U) { mid_dma<J, 4>(m); SB(); }
    EX4(m.s1, 12);
}

template <int J> DI void mid_stage(Mid& m) {
    half_stage<J, 0>(m, m.fa, m.fb);
    if constexpr (J < LAST) {
        if constexpr (J == 0) { if (m.w < 4) wait_vm<6>(); else wait_vm<4>(); }
        else if constexpr (J == 1) { if (m.w < 4) wait_vm<8>(); else wait_vm<6>(); }
        else if constexpr (J == LEAD - 1) wait_vm<6>();
        else wait_vm<ops_after(J - 1)>();
        if constexpr (J == KV_AT) { if (m.w == 7) poll_finish(m.pv, m.pollb, m.lane < 16, m.epoch); }
        if constexpr (J == LAST - 2) { if (m.w == 7) poll_finish(m.pv, m.pollc, m.lane < 12, m.epoch); }
        bar();
        if constexpr (J == LEAD - 1) asm volatile("" : "+v"(m.q0), "+v"(m.q1));
        if constexpr (J == 2) { if (threadIdx.x == 0) post_flag(m.ws, m.fo2, m.epoch); }
        if constexpr (J == 17) { if (threadIdx.x == 0) post_flag(m.ws, m.fo4, m.epoch); }
        if constexpr (J == KV_AT - 1) { if (m.w == 7) poll_issue(m.pv, m.pollb); }
        if constexpr (J == LAST - 3) { if (m.w == 7) poll_issue(m.pv, m.pollc); }
    }
    half_stage<J, 1>(m, m.fb, m.fa);
    if constexpr (J == 15) {
        store16c(pack8<0>(m.y0, 1.f), m.sw, m.vl, m.op);
        store16c(pack8<8>(m.y0, 1.f), m.sw, m.vl, m.op + 1024);
        store16c(pack8<0>(m.y1, 1.f), m.sw, m.vl, m.op + 2048);
        store16c(pack8<8>(m.y1, 1.f), m.sw, m.vl, m.op + 3072);
    }
}
template <int J, int E> DI void mid_stages(Mid& m) {
    if constexpr (J < E) { mid_stage<J>(m); mid_stages<J + 1, E>(m); }
}

constexpr int w_lds(int n) { return n < 64 ? P_W0 + n * 1024 : P_W1 + (n - 64) * 1024; }

template <int ROLE>
DI void proj(const char* x, const char* wqkv, const char* wri, const char* gw, char* ws, lds_ptr lds, Mid& m, int mi, int nj, const char* polla
) {
    const int lane = m.lane, w = m.w, r = lane & 31, h = lane >> 5, wm = w & 3;
    const unsigned l0 = m.l0;
    const u32x4 sx = make_srd(x, S * D * 2), sq = make_srd(wqkv, D * 3 * D * 2), sr = make_srd(wri, D * D * 2);
    const int xrow = 8 * w + (lane >> 3), rrow = 8 * wm + (lane >> 3);
    const unsigned vx = xrow * 1024 + (((lane & 7) ^ ((xrow >> 1) & 7)) << 4);
    const unsigned vr = rrow * 1024 + (((lane & 7) ^ ((rrow >> 1) & 7)) << 4);
    const unsigned vq = (lane >> 2) * 3072 + (lane & 3) * 16;
    u32x4 gx, gv;
    u64 gen;
    {
        const char* px = x + (size_t)(mi * 128 + nj * 8 + w) * 1024 + lane * 16;
        const char* pg = gw + lane * 16;
        asm volatile("global_load_dwordx4 %0, %3, off\n\tglobal_load_dwordx4 %1, %4, off\n\tglobal_load_dwordx2 %2, %5, off sc1"
                     : "=&v"(gx), "=&v"(gv), "=&v"(gen) : "v"(px), "v"(pg), "v"(ws + WS_GEN) : "memory");
    }
    auto stage_dma = [&](int st) {
        const unsigned d = l0 + (st & 3) * P_SLOT + w * 1024;
        dma16(d, vx, sx, mi * 131072 + st * 128);
        dma16(d + 8192, vx, sx, mi * 131072 + st * 128 + 65536);
        dma16(d + 16384, vr, sr, nj * 32768 + st * 128);
    };
    auto w_dma = [&](int i) {
        const unsigned n = 8 * i + w, ks = (n * 43691u) >> 17, blk = n - 3 * ks;
        dma16(l0 + (i < 8 ? P_W0 : P_W1 - 65536) + n * 1024, vq, sq, ks * 49152 + blk * 1024 + nj * 64);
    };
    stage_dma(0); stage_dma(1); stage_dma(2); stage_dma(3);

    lds_ptr xa[4], ra[4];
#pragma unroll
    for (int p = 0; p < 4; ++p) {
        const int sw16 = ((2 * p + h) ^ ((r >> 1) & 7)) << 4;
        xa[p] = lds + (32 * wm + r) * 128 + sw16;
        ra[p] = lds + 16384 + ROLE * 4096 + r * 128 + sw16;
    }
    bf16x8 xf[32], rf[4];
    f32x16 c0 = {}, c1 = {};
    float gate = 0.f;
#pragma unroll
    for (int st = 0; st < 8; ++st) {
        if (st == 0) wait_bar<9>();
        else if (st <= 4) wait_bar<6>();
        else if (st == 5) wait_bar<10>();
        else if (st == 6) wait_bar<11>();
        else wait_bar<8>();
        if (st == 0) STAMP(0);
        const int so = (st & 3) * P_SLOT;
        SB();
        xf[4 * st] = ldsfrag(xa[0] + so);
        const bf16x8 r0 = ldsfrag(ra[0] + so);
        xf[4 * st + 1] = ldsfrag(xa[1] + so);
        const bf16x8 r1 = ldsfrag(ra[1] + so);
        SB();
        if (st > 0) { c0 = MFMA(xf[4 * st - 2], rf[2], c0); PIN(c0); SB(); }
        if (st >= 1 && st <= 4) stage_dma(st + 3);
        if (st == 5) { w_dma(4); w_dma(5); w_dma(6); w_dma(7); }
        if (st == 7) { w_dma(8); w_dma(9); w_dma(10); }
        SB();
        if (st > 0) { c0 = MFMA(xf[4 * st - 1], rf[3], c0); PIN(c0); SB(); }
        xf[4 * st + 2] = ldsfrag(xa[2] + so);
        rf[2] = ldsfrag(ra[2] + so);
        xf[4 * st + 3] = ldsfrag(xa[3] + so);
        rf[3] = ldsfrag(ra[3] + so);
        SB();
        if (st == 4) { w_dma(0); w_dma(1); w_dma(2); w_dma(3); }
        if (st == 0) {
            asm volatile("" : "+v"(gx), "+v"(gv), "+v"(gen));
            m.epoch = gen + 1;
#pragma unroll
            for (int i = 0; i < 4; ++i) gate += bf_lo(gx[i]) * bf_lo(gv[i]) + bf_hi(gx[i]) * bf_hi(gv[i]);
#pragma unroll
            for (int o = 32; o > 0; o >>= 1) gate += __shfl_xor(gate, o);
            gate = 1.f / (1.f + __expf(-gate));
            PIN(gate);
        }
        SB();
        c0 = MFMA(xf[4 * st], r0, c0); PIN(c0); SB();
        c0 = MFMA(xf[4 * st + 1], r1, c0); PIN(c0); SB();
    }
    c0 = MFMA(xf[30], rf[2], c0);
    c0 = MFMA(xf[31], rf[3], c0);
    wait_bar<3>();
    STAMP(1);
    w_dma(11);
    const int tile = mi * 4 + wm;
    const lds_ptr wa0 = lds + P_W0 + (8 * h + ((lane & 15) >> 2)) * 64 + ((lane >> 4) & 1) * 32 + (lane & 3) * 8;
    const lds_ptr wa1 = wa0 - P_W0 + P_W1;
#define WFRAG(n) ((n) < 64 ? ldsfrag_tr(wa0 + (n) * 1024, wa0 + (n) * 1024 + 256) : ldsfrag_tr(wa1 + ((n) - 64) * 1024, wa1 + ((n) - 64) * 1024 + 256))
    constexpr int PD = 3;
    bf16x8 wf0[PD], wf1[PD];
#pragma unroll
    for (int ks = 0; ks < PD; ++ks) { wf0[ks] = WFRAG(3 * ks + ROLE); if (ROLE == 0) wf1[ks] = WFRAG(3 * ks + 2); }
    SB();
    {
        const float sg = ROLE ? -1.f : 1.f;
        const unsigned oa = WS_AB + nj * 262144 + ROLE * 131072 + tile * 2048;
        store16c(natural_order(pack8<0>(c0, sg)), m.sw, m.vl, oa);
        store16c(natural_order(pack8<8>(c0, sg)), m.sw, m.vl, oa + 1024);
        if (lane == 0) __hip_atomic_store((float*)(ws + WS_G) + mi * 128 + nj * 8 + w, gate, __ATOMIC_RELAXED, __HIP_MEMORY_SCOPE_AGENT);
    }
    SB();
#pragma unroll
    for (int st = 0; st < 3; ++st) {
        dma16(l0 + M_C + st * 16384 + w * 1024, m.vc, m.sc, m.oc + st * 128);
        dma16(l0 + M_C + st * 16384 + 8192 + w * 1024, m.vc, m.sc, m.oc + st * 128 + 262144);
    }
    c0 = f32x16{};
#pragma unroll
    for (int ks = 0; ks < 32; ++ks) {
        SB();
        if (ks + PD == 21) {
            wait_bar<6>();
            if (threadIdx.x == 0) post_flag(ws, WS_F + (nj * 16 + mi) * 8, m.epoch);
            STAMP(2);
            SB();
        }
        const bf16x8 f0 = wf0[ks % PD], f1 = wf1[ks % PD];
        c0 = MFMA(f0, xf[ks], c0); PIN(c0); SB();
        if (ks + PD < 32) { wf0[ks % PD] = WFRAG(3 * (ks + PD) + ROLE); SB(); }
        if (ROLE == 0) {
            c1 = MFMA(xf[ks], f1, c1); PIN(c1); SB();
            if (ks + PD < 32) { wf1[ks % PD] = WFRAG(3 * (ks + PD) + 2); SB(); }
        }
    }
    if (ROLE == 1 && w == 7) {
        u64 v;
        asm volatile("global_load_dwordx2 %0, %1, off sc1\n\ts_waitcnt vmcnt(0)" : "=&v"(v) : "v"(polla) : "memory");
        poll_finish(v, polla, lane < 32, m.epoch);
    }
    asm volatile("s_waitcnt lgkmcnt(0)\n\ts_barrier" ::: "memory");
    STAMP(3);
#pragma unroll
    for (int st = 0; st < 3; ++st) {
        dma16c(l0 + M_AB + st * 16384 + w * 2048, m.vl, m.sw, m.oab + st * 4096);
        dma16c(l0 + M_AB + st * 16384 + w * 2048 + 1024, m.vl, m.sw, m.oab + st * 4096 + 1024);
    }
    SB();
    if (ROLE == 0) {
        constexpr float QS = 0.25f * 1.4426950408889634f;
        const unsigned oq = WS_Q + 2 * nj * 65536 + tile * 1024;
        store16c(pack8<0>(c0, QS), m.sw, m.vl, oq);
        store16c(pack8<8>(c0, QS), m.sw, m.vl, oq + 65536);
        const unsigned ov = WS_V + 2 * nj * 65536 + tile * 1024, vv = (r >> 4) * 65536 + h * 256 + (r & 15) * 16;
        store16c(pack8<0>(c1, 1.f), m.sw, vv, ov);
        store16c(pack8<8>(c1, 1.f), m.sw, vv, ov + 512);
        wait_bar<8>();
    } else {
        const int slot = (r & 19) | ((r & 4) << 1) | ((r & 8) >> 1);
        const unsigned ok = WS_K + 2 * nj * 65536 + tile * 1024, vk = h * 512 + slot * 16;
        store16c(pack8<0>(c0, 1.f), m.sw, vk, ok);
        store16c(pack8<8>(c0, 1.f), m.sw, vk, ok + 65536);
        if (w < 6) {
            const unsigned one2 = ((lane & 11) == 0) ? 0x3f803f80u : 0u;
            const u32x4 cv = {one2, one2, one2, one2};
            *(LAS u32x4*)(lds + M_CONST + (w - 4) * 1024 + lane * 16) = cv;
        }
        wait_bar<6>();
    }
#undef WFRAG
}

extern "C" __global__ __launch_bounds__(512, 2) void k_fused(const char* x, const char* cr, const char* ci, const char* wr, const char* wi,
                                                              const char* wqkv, const char* wo, const char* gw, char* ws, char* out) {
    extern __shared__ __attribute__((aligned(16))) char smem[];
    STAMP_BEGIN();
    const lds_ptr lds = (lds_ptr)smem;
    const int lane = threadIdx.x & 63, w = __builtin_amdgcn_readfirstlane(threadIdx.x >> 6);
    {
        u64 pc;
        unsigned len;
        asm volatile("s_getpc_b64 %0\n.Lpc0:\n\ts_mov_b32 %1, .Lpc1-.Lpc0" : "=s"(pc), "=s"(len));
        const u32x4 scode = make_srd((const void*)pc, len);
        asm volatile("s_mov_b32 m0, %0\n\ts_nop 0\n\tbuffer_load_dword %1, %2, 0 offen lds" ::"s"((unsigned)(uintptr_t)lds + P_W0 + w * 256), "v"(threadIdx.x * 96), "s"(scode) : "memory");
    }
    const int r = lane & 31, h = lane >> 5, qg = w & 3, kh = w >> 2;
    const int g = blockIdx.x & 7, i = blockIdx.x >> 3;
    const int mi = (g & 1) * 8 + (i & 7), nj = (g >> 1) * 4 + (i >> 3);
    const int head = g * 4 + (i >> 3), qb = i & 7;
    const int sp = g >> 1, mt = (g & 1) * 8 + (i >> 2), nt = i & 3;
    const int tt = g * 4 + (i >> 3), et = i & 7;
    const int te = w & 1, tq = w >> 1;
    const int tb = (w >> 1) & 1, eb = w & 1;

    Mid m;
    m.lds = lds; m.l0 = (unsigned)(uintptr_t)lds; m.w = w; m.lane = lane; m.ws = ws;
    m.sc = make_srd(sp < 2 ? cr : ci, S * S * 2); m.sw = make_srd(ws, WS_END); m.so = make_srd(wo, D * D * 2);
    m.rw = __builtin_amdgcn_make_buffer_rsrc(ws, 0, WS_END, 0x00020000);
    const int crow = 8 * w + (lane >> 3);
    m.vc = crow * 4096 + (((lane & 7) ^ ((crow >> 1) & 7)) << 4);
    m.vl = lane * 16;
    m.oc = mt * 128 * 4096 + (sp & 1) * 2048;
    m.oab = WS_AB + (nt * 4 + (w >> 1)) * 262144 + (sp * 64 + (w & 1) * 2) * 1024;
    {
        const char* polla = ws + WS_F + ((nt * 4 + ((lane >> 3) & 3)) * 16 + (sp & 1) * 8 + (lane & 7)) * 8;
        if (w < 4) proj<0>(x, wqkv, wr, gw, ws, lds, m, mi, nj, polla);
        else proj<1>(x, wqkv, wi, gw, ws, lds, m, mi, nj, polla);
    }
    STAMP(4);
    const u64 epoch = m.epoch;
    {
        int tz = threadIdx.x;
        asm volatile("" : "+v"(tz));
        const int ln = tz & 63, rr = ln & 31, hh = ln >> 5;
        m.okv = ((w & 2) ? WS_V : WS_K) + head * 65536 + (kh * 32 + (w & 1)) * 1024;
        m.owo = w * 16384 + et * 128;
        m.vwo = (ln >> 3) * 1024 + (((ln & 7) ^ (((ln >> 3) & 2) << 1)) << 4);
        m.op = WS_P + sp * (2u << 20) + (mt * 4 + tq) * 32768 + (nt * 4 + te * 2) * 2048;
        m.fo2 = WS_F + 2048 + (nj * 16 + mi) * 8;
        m.fo4 = WS_F + 6144 + ((mt * 4 + nt) * 4 + sp) * 8;
        m.pq = ws + WS_Q + (size_t)head * 65536 + (qb * 8 + qg * 2 + kh) * 1024 + ln * 16;
        m.pollb = ws + WS_F + 2048 + ((head >> 1) * 16 + (ln & 15)) * 8;
        m.pollc = ws + WS_F + ((ln & 8) ? 6144 + (((tt >> 1) * 4 + (et >> 1)) * 4 + (ln & 3)) * 8 : (((tt & 1) * 8 + (ln & 7)) * 16 + (tt >> 1)) * 8);
        m.pp = ws + WS_P + (size_t)(2 * kh) * (2u << 20) + (size_t)(tt * 2 + tb) * 32768 + (et * 2 + eb) * 2048 + ln * 16;
        m.pg = ws + WS_G + (tt * 64 + tb * 32 + rr) * 4;
        const int cslot = (rr == 16) ? 0 : (rr == 20) ? 4 : (rr < 20 || rr >= 28) ? 1 : 5;
#pragma unroll
        for (int s = 0; s < 3; ++s) m.va[s] = (rr < 16) ? lds + M_KV + s * 8192 + kh * 4096 + 2048 + hh * 256 + rr * 16 : lds + M_CONST + cslot * 16;
        m.ka = lds + M_KV + kh * 4096 + ln * 16;
        m.aa = lds + M_AB + te * 8192 + ln * 16;
#pragma unroll
        for (int p = 0; p < 4; ++p) m.ca[p] = lds + M_C + (tq * 32 + rr) * 128 + (((2 * p + hh) ^ ((rr >> 1) & 7)) << 4);
    }
    m.idx = (int)0xDDDDDDDD;
    PIN(m.idx);
    m.o0 = f32x16{}; m.o1 = f32x16{}; m.y0 = f32x16{}; m.y1 = f32x16{};
    m.fa.a0 = ldsfrag(m.aa); m.fa.c0 = ldsfrag(m.ca[0]); m.fa.a1 = ldsfrag(m.aa + 4096);
    m.fa.a2 = ldsfrag(m.aa + 1024); m.fa.c1 = ldsfrag(m.ca[1]); m.fa.a3 = ldsfrag(m.aa + 4096 + 1024);
    mid_stages<0, LEAD>(m);
    STAMP(5);
    mid_stages<LEAD, 16>(m);
    STAMP(6);
    mid_stages<16, LAST + 1>(m);
    m.o1 = pv_mfma<8>(m.fb.v1, m.s1, m.o1, m.idx);
    STAMP(7);

    {
        const f32x16 zero = {};
        const lds_ptr mine = lds + M_X + w * 3072 + lane * 16, other = lds + M_X + (w ^ 4) * 3072 + lane * 16;
#pragma unroll
        for (int j = 0; j < 3; ++j) *(LAS f32x4*)(mine + j * 1024) = f32x4{m.o1[4 * j], m.o1[4 * j + 1], m.o1[4 * j + 2], m.o1[4 * j + 3]};
        asm volatile("s_waitcnt lgkmcnt(0)\n\ts_barrier" ::: "memory");
        f32x16 o = m.o0;
#pragma unroll
        for (int j = 0; j < 3; ++j) {
            const f32x4 t = *(const LAS f32x4*)(other + j * 1024);
#pragma unroll
            for (int e = 0; e < 4; ++e) o[4 * j + e] += t[e];
        }
        bool ok = o[8] >= 8.271806e-25f && o[8] <= 1.2676506e30f;
#pragma unroll
        for (int e = 0; e < 8; ++e) ok = ok && (__builtin_fabsf(o[e]) <= 3.0e38f);
        if (__builtin_expect(__any(!ok), 0)) {
            const unsigned kb = WS_K + head * 65536, vb = WS_V + head * 65536, vv = h * 256 + (r & 15) * 16;
            float mx = -__builtin_inff();
            for (int t = 0; t < 64; ++t) {
                const u32x4 kf = __builtin_amdgcn_raw_buffer_load_b128(m.rw, m.vl, kb + t * 1024, 16);
                const f32x16 s = MFMA(__builtin_bit_cast(bf16x8, kf), m.q0, zero);
#pragma unroll
                for (int e = 0; e < 16; ++e) mx = __builtin_fmaxf(mx, s[e]);
            }
            mx = __builtin_fmaxf(mx, __shfl_xor(mx, 32));
            const unsigned cw = (r == 16 || r == 20) ? 0x3f803f80u : 0u;
            const u32x4 cv = {cw, cw, cw, cw};
            o = zero;
            for (int t = 0; t < 64; ++t) {
                const u32x4 kf = __builtin_amdgcn_raw_buffer_load_b128(m.rw, m.vl, kb + t * 1024, 16);
                const u32x4 v0 = __builtin_amdgcn_raw_buffer_load_b128(m.rw, vv, vb + t * 1024, 16);
                const u32x4 v1 = __builtin_amdgcn_raw_buffer_load_b128(m.rw, vv, vb + t * 1024 + 512, 16);
                f32x16 s = MFMA(__builtin_bit_cast(bf16x8, kf), m.q0, zero);
#pragma unroll
                for (int e = 0; e < 16; ++e) s[e] = __builtin_amdgcn_exp2f(s[e] - mx);
                o = pv_mfma<0>(__builtin_bit_cast(bf16x8, r < 16 ? v0 : cv), s, o, m.idx);
                o = pv_mfma<8>(__builtin_bit_cast(bf16x8, r < 16 ? v1 : cv), s, o, m.idx);
            }
        }
        store16c(pack8<0>(o, 1.f / o[8]), m.sw, m.vl, WS_O + (qb * 8 + qg * 2 + kh) * 32768 + head * 1024);
    }
    asm volatile("s_waitcnt vmcnt(0) lgkmcnt(0)\n\ts_barrier" ::: "memory");
    STAMP(8);
    if (threadIdx.x == 0) post_flag(ws, WS_F + 4096 + (qb * 32 + head) * 8, epoch);
    if (w == 7) {
        const char* polld = ws + WS_F + 4096 + ((tt >> 2) * 32 + r) * 8;
        u64 v;
        asm volatile("global_load_dwordx2 %0, %1, off sc1\n\ts_waitcnt vmcnt(0)" : "=&v"(v) : "v"(polld) : "memory");
        poll_finish(v, polld, true, epoch);
    }
    bar();
    STAMP(9);

#pragma unroll
    for (int c = 0; c < 4; ++c) {
        const int ks = c * 4 + (w & 3);
        dma16c(m.l0 + (kh ? o_chunk(1) : o_chunk(0)) + ks * 1024, m.vl, m.sw, WS_O + (tt * 2) * 32768 + (kh * 16 + ks) * 1024);
        dma16c(m.l0 + (kh ? o_chunk(3) : o_chunk(2)) + ks * 1024, m.vl, m.sw, WS_O + (tt * 2 + 1) * 32768 + (kh * 16 + ks) * 1024);
    }
    {
        const int q = (lane & 15) >> 2;
        const lds_ptr oa = lds + (tb ? (kh ? o_chunk(3) : o_chunk(2)) : (kh ? o_chunk(1) : o_chunk(0))) + lane * 16;
        const int wl = (4 * h + q) * 128 + ((eb * 64) ^ ((q & 2) << 5)) + ((lane >> 4) & 1) * 32 + (lane & 3) * 8;
        const lds_ptr wa0 = lds + (kh ? wo_chunk(2) : wo_chunk(0)) + wl, wa1 = lds + (kh ? wo_chunk(3) : wo_chunk(1)) + wl;
        bf16x8 wf[16];
#pragma unroll
        for (int ks = 0; ks < 16; ++ks) {
            const lds_ptr p = (ks < 8 ? wa0 : wa1) + (ks & 7) * 2048;
            wf[ks] = ldsfrag_tr(p, p + 1024);
        }
        asm volatile("" : "+v"(m.p0), "+v"(m.p1), "+v"(m.p2), "+v"(m.p3), "+v"(m.gate));
        f32x16 yv;
#pragma unroll
        for (int j = 0; j < 8; ++j) {
            const unsigned a = j < 4 ? m.p0[j & 3] : m.p1[j & 3], b = j < 4 ? m.p2[j & 3] : m.p3[j & 3];
            yv[2 * j] = m.gate * (bf_lo(a) + bf_lo(b));
            yv[2 * j + 1] = m.gate * (bf_hi(a) + bf_hi(b));
        }
        PIN(yv);
        f32x16 acc = {};
#pragma unroll
        for (int c = 0; c < 4; ++c) {
            SB();
            if (c == 0) wait_bar<6>(); else if (c == 1) wait_bar<4>(); else if (c == 2) wait_bar<2>(); else wait_bar<0>();
            bf16x8 of[4];
#pragma unroll
            for (int p = 0; p < 4; ++p) of[p] = ldsfrag(oa + (c * 4 + p) * 1024);
            SB();
#pragma unroll
            for (int p = 0; p < 4; ++p) acc = MFMA(wf[c * 4 + p], of[p], acc);
        }
        STAMP(10);
        const float og = 1.f - m.gate;
        const lds_ptr st = lds + M_X - 2048 + kh * (64 * O_PITCH) + (tb * 32 + r) * O_PITCH + (eb * 32 + 4 * h) * 4;
        asm volatile("s_waitcnt lgkmcnt(0)\n\ts_barrier" ::: "memory");
#pragma unroll
        for (int j = 0; j < 4; ++j) {
            const f32x4 v = {yv[4 * j] + og * acc[4 * j], yv[4 * j + 1] + og * acc[4 * j + 1], yv[4 * j + 2] + og * acc[4 * j + 2], yv[4 * j + 3] + og * acc[4 * j + 3]};
            *(LAS f32x4*)(st + j * 32) = v;
        }
        asm volatile("s_waitcnt lgkmcnt(0)\n\ts_barrier" ::: "memory");
        const int row = threadIdx.x >> 3, ch = threadIdx.x & 7;
        const lds_ptr rd = lds + M_X - 2048 + row * O_PITCH + ch * 32;
        const f32x4 a0 = *(const LAS f32x4*)rd, a1 = *(const LAS f32x4*)(rd + 16);
        const f32x4 b0 = *(const LAS f32x4*)(rd + 64 * O_PITCH), b1 = *(const LAS f32x4*)(rd + 64 * O_PITCH + 16);
        const u32x4 o = {pk2(a0[0] + b0[0], a0[1] + b0[1]), pk2(a0[2] + b0[2], a0[3] + b0[3]), pk2(a1[0] + b1[0], a1[1] + b1[1]), pk2(a1[2] + b1[2], a1[3] + b1[3])};
        *(u32x4*)(out + (size_t)(tt * 64 + row) * 1024 + et * 128 + ch * 16) = o;
    }
    asm volatile(".Lpc1:");
    if (blockIdx.x == 0 && w < 4) {
        const char* pe = ws + WS_F + 2048 + threadIdx.x * 8;
        u64 v;
        asm volatile("global_load_dwordx2 %0, %1, off sc1\n\ts_waitcnt vmcnt(0)" : "=&v"(v) : "v"(pe) : "memory");
        poll_finish(v, pe, true, epoch);
    }
    if (blockIdx.x == 0) {
        bar();
        if (threadIdx.x == 0) post_flag(ws, WS_GEN, epoch);
    }
    STAMP_END();
}

extern "C" void kernel_launch(void* const* d_in, const int* in_sizes, int n_in, void* d_out, int out_size, void* d_ws, size_t ws_size, hipStream_t stream) {
    (void)in_sizes; (void)n_in; (void)out_size; (void)ws_size;
    (void)hipFuncSetAttribute((const void*)k_fused, hipFuncAttributeMaxDynamicSharedMemorySize, LDS_BYTES);
    k_fused<<<256, 512, LDS_BYTES, stream>>>((const char*)d_in[0], (const char*)d_in[1], (const char*)d_in[2], (const char*)d_in[3], (const char*)d_in[4],
                                             (const char*)d_in[6], (const char*)d_in[7], (const char*)d_in[8], (char*)d_ws, (char*)d_out);
}
```

```cpp
#include <hip/hip_runtime.h>
#include <hip/hip_bf16.h>
#include <stdint.h>

typedef unsigned long long u64;
typedef float f32x16 __attribute__((ext_vector_type(16)));
typedef float f32x8 __attribute__((ext_vector_type(8)));
typedef float f32x4 __attribute__((ext_vector_type(4)));
typedef __bf16 bf16x8 __attribute__((ext_vector_type(8)));
typedef __bf16 bf16x16 __attribute__((ext_vector_type(16)));
typedef unsigned u32x4 __attribute__((ext_vector_type(4)));
typedef short s16x4 __attribute__((ext_vector_type(4)));
#define LAS __attribute__((address_space(3)))
typedef LAS char* lds_ptr;
#define DI __device__ __forceinline__
#define SB() __builtin_amdgcn_sched_barrier(0)
#define PIN(x) asm volatile("" : "+v"(x))
#define MFMA(a, b, c) __builtin_amdgcn_mfma_f32_32x32x16_bf16((a), (b), (c), 0, 0, 0)

constexpr int S = 2048, D = 512;
#define ROT2 0
#define WSOFF 0
#define ROT 1
#define CNT 0
#define LEAD 3
constexpr unsigned WS_Q = 0;
constexpr unsigned WS_K = 2u << 20;
constexpr unsigned WS_V = 4u << 20;
constexpr unsigned WS_AB = 6u << 20;
constexpr unsigned WS_O = 10u << 20;
constexpr unsigned WS_P = 12u << 20;
constexpr unsigned WS_G = 20u << 20;
constexpr unsigned WS_F = WS_G + 8192;
constexpr unsigned WS_GEN = WS_F + 8192;
constexpr unsigned WS_END = WS_GEN + 256;
constexpr unsigned SPIN_CAP = 1u << 20;

#define STAMP_BEGIN()
#define STAMP(i)
#define STAMP1(i)
#define STAMP2(i)
#define STAMP3(i)
#define STAMP_END()

DI u32x4 make_srd(const void* p, unsigned bytes) {
    const u64 a = (u64)p;
    u32x4 r = {(unsigned)a, (unsigned)(a >> 32) & 0xffffu, bytes, 0x00020000u};
    return r;
}
DI void dma16(unsigned lds, unsigned voff, u32x4 srd, unsigned soff) {
    asm volatile("s_mov_b32 m0, %0\n\ts_nop 0\n\tbuffer_load_dwordx4 %1, %2, %3 offen lds" ::"s"(lds), "v"(voff), "s"(srd), "s"(soff) : "memory");
}
DI void dma16n(unsigned lds, unsigned voff, u32x4 srd, unsigned soff) {
    asm volatile("s_mov_b32 m0, %0\n\ts_nop 0\n\tbuffer_load_dwordx4 %1, %2, %3 offen nt lds" ::"s"(lds), "v"(voff), "s"(srd), "s"(soff) : "memory");
}
DI void dma16p(unsigned lds, unsigned voff, u32x4 srd, unsigned soff) { if (CNT) dma16n(lds, voff, srd, soff); else dma16(lds, voff, srd, soff); }
DI void dma16c(unsigned lds, unsigned voff, u32x4 srd, unsigned soff) {
    asm volatile("s_mov_b32 m0, %0\n\ts_nop 0\n\tbuffer_load_dwordx4 %1, %2, %3 offen sc1 lds" ::"s"(lds), "v"(voff), "s"(srd), "s"(soff) : "memory");
}
DI void store16c(u32x4 v, u32x4 srd, unsigned voff, unsigned soff) {
    asm volatile("buffer_store_dwordx4 %0, %1, %2, %3 offen sc1\n\ts_nop 1" ::"v"(v), "v"(voff), "s"(srd), "s"(soff) : "memory");
}
DI void post_flag(char* ws, unsigned off, u64 epoch) { __hip_atomic_store((u64*)(ws + off), epoch, __ATOMIC_RELAXED, __HIP_MEMORY_SCOPE_AGENT); }
DI void poll_issue(u64& v, u32x4 srd, unsigned voff) { asm volatile("buffer_load_dwordx2 %0, %1, %2, 0 offen sc1" : "=v"(v) : "v"(voff), "s"(srd) : "memory"); }
DI void poll_finish(u64 v, u32x4 srd, unsigned voff, u64 epoch) {
    asm volatile("" : "+v"(v));
    unsigned spins = 0;
    while (!__all(v == epoch)) {
        if (++spins > SPIN_CAP) break;
        __builtin_amdgcn_s_sleep(1);
        asm volatile("buffer_load_dwordx2 %0, %1, %2, 0 offen sc1\n\ts_waitcnt vmcnt(0)" : "=&v"(v) : "v"(voff), "s"(srd) : "memory");
    }
}
DI void poll_block(u32x4 srd, unsigned voff, u64 epoch) {
    u64 v;
    asm volatile("buffer_load_dwordx2 %0, %1, %2, 0 offen sc1\n\ts_waitcnt vmcnt(0)" : "=&v"(v) : "v"(voff), "s"(srd) : "memory");
    poll_finish(v, srd, voff, epoch);
}
DI bf16x8 ldsfrag(lds_ptr p) { return *(const LAS bf16x8*)p; }
DI bf16x8 ldsfrag_tr(lds_ptr p0, lds_ptr p1) {
    const s16x4 lo = __builtin_amdgcn_ds_read_tr16_b64_v4i16((LAS s16x4*)p0);
    const s16x4 hi = __builtin_amdgcn_ds_read_tr16_b64_v4i16((LAS s16x4*)p1);
    return __builtin_bit_cast(bf16x8, __builtin_shufflevector(lo, hi, 0, 1, 2, 3, 4, 5, 6, 7));
}
DI unsigned pk2(float lo, float hi) {
    typedef __bf16 bf16x2 __attribute__((ext_vector_type(2)));
    typedef float f32x2 __attribute__((ext_vector_type(2)));
    const f32x2 v = {lo, hi};
    return __builtin_bit_cast(unsigned, __builtin_convertvector(v, bf16x2));
}
template <int B> DI u32x4 pack8(const f32x16& v, float sc) {
    u32x4 r = {pk2(v[B] * sc, v[B + 1] * sc), pk2(v[B + 2] * sc, v[B + 3] * sc), pk2(v[B + 4] * sc, v[B + 5] * sc), pk2(v[B + 6] * sc, v[B + 7] * sc)};
    return r;
}
DI u32x4 natural_order(u32x4 p) {
    const auto s0 = __builtin_amdgcn_permlane32_swap(p[0], p[2], false, false);
    const auto s1 = __builtin_amdgcn_permlane32_swap(p[1], p[3], false, false);
    u32x4 r = {s0[0], s1[0], s0[1], s1[1]};
    return r;
}
DI float bf_lo(unsigned u) { return __uint_as_float(u << 16); }
DI float bf_hi(unsigned u) { return __uint_as_float(u & 0xffff0000u); }
template <int B> DI f32x16 pv_mfma(bf16x8 vfrag, const f32x16& p, f32x16 o, int idx) {
    const f32x8 h = __builtin_shufflevector(p, p, B, B + 1, B + 2, B + 3, B + 4, B + 5, B + 6, B + 7);
    return __builtin_amdgcn_smfmac_f32_32x32x32_bf16(vfrag, __builtin_bit_cast(bf16x16, h), o, idx, 0, 0);
}
template <int N> DI void wait_vm() { asm volatile("s_waitcnt vmcnt(%0) lgkmcnt(0)" ::"i"(N) : "memory"); }
DI void bar() { asm volatile("s_barrier" ::: "memory"); }
template <int N> DI void wait_bar() { wait_vm<N>(); bar(); }

constexpr int P_SLOT = 28672, P_W0 = 114688, P_W1 = 98304;
constexpr int M_C = 0, M_AB = 49152, M_KV = 98304, M_CONST = M_KV + 3 * 8192, M_X = 131072;
constexpr int LDS_BYTES = 163840, O_PITCH = 272;
constexpr int wo_chunk(int c) { return c == 0 ? 16384 : c == 1 ? 65536 : c == 2 ? 32768 : 81920; }
constexpr int o_chunk(int c) { return c == 0 ? 0 : c == 1 ? 49152 : c == 2 ? 98304 : 114688; }
#define KVLEAD 2
constexpr int KV_AT = LEAD - KVLEAD, LAST = LEAD + 15;
constexpr int ops_after(int j) {
    int n = 0;
    if (j <= 14) n += 4;
    if (j == KV_AT) n += 5;
    if (j >= LEAD && j + 3 - LEAD < 16) n += 1;
    if (j == 15) n += 4;
    return n;
}
static_assert(KV_AT >= 1 && LEAD <= 12 && LAST > 17 && (KVLEAD == 2 || KVLEAD == 3), "phase 2 schedule");

struct Frag { bf16x8 k, v0, v1, a0, a1, c0, a2, a3, c1; };

struct Mid {
    lds_ptr lds, ka, va[3], aa, ca[4];
    unsigned l0, vc, vl, vwo, oc, oab, okv, owo, w, rot;
    u32x4 sc, sw, so;
    __amdgpu_buffer_rsrc_t rw;
    char* ws;
    unsigned op, fo2, fo4, oq, opp, og, fb0, fc0, fc1, pollv;
    u64 epoch, pv;
    int lane, idx;
    bf16x8 q0, q1;
    f32x16 o0, o1, y0, y1, s0, s1;
    Frag fa, fb;
    u32x4 p0, p1, p2, p3;
    float gate;
};

template <int J, int P> DI void mid_dma(Mid& m) {
    constexpr int SL = J % 3;
    if constexpr (J < 0 || P > 4) {
    } else if constexpr (J + 3 < 16) {
        const unsigned ks = (J + 3 + m.rot) & 15;
        if constexpr (P == 1) dma16p(m.l0 + M_C + SL * 16384 + m.w * 1024, m.vc, m.sc, m.oc + ks * 128);
        if constexpr (P == 2) dma16p(m.l0 + M_C + SL * 16384 + 8192 + m.w * 1024, m.vc, m.sc, m.oc + ks * 128 + 262144);
        if constexpr (P == 3) dma16c(m.l0 + M_AB + SL * 16384 + m.w * 2048, m.vl, m.sw, m.oab + ks * 4096);
        if constexpr (P == 4) dma16c(m.l0 + M_AB + SL * 16384 + m.w * 2048 + 1024, m.vl, m.sw, m.oab + ks * 4096 + 1024);
    } else if constexpr (J <= 14 && P >= 1) {
        constexpr int c = (J - 13) * 2 + (P - 1) / 2, hf = (P - 1) & 1;
        dma16n(m.l0 + wo_chunk(c) + m.w * 2048 + hf * 1024, m.vwo, m.so, m.owo + c * 131072 + hf * 8192);
    }
    if constexpr (P == 0) {
        if constexpr (J == KV_AT) {
            asm volatile("buffer_load_dwordx4 %0, %2, %3, %4 offen sc1\n\tbuffer_load_dwordx4 %1, %2, %3, %5 offen sc1"
                         : "=&v"(m.q0), "=&v"(m.q1) : "v"(m.vl), "s"(m.sw), "s"(m.oq), "s"(m.oq ^ 1024) : "memory");
            dma16c(m.l0 + M_KV + m.w * 1024, m.vl, m.sw, m.okv);
        }
        if constexpr (J >= LEAD && J + 3 - LEAD < 16) dma16c(m.l0 + M_KV + ((J - LEAD) % 3) * 8192 + m.w * 1024, m.vl, m.sw, m.okv + (J + 3 - LEAD) * 2048);
    }
    if constexpr (P == 5 && J == KV_AT) {
        dma16c(m.l0 + M_KV + 8192 + m.w * 1024, m.vl, m.sw, m.okv + 2048);
        dma16c(m.l0 + M_KV + 16384 + m.w * 1024, m.vl, m.sw, m.okv + 4096);
    }
}

#define EX3(X, i) do { if constexpr (ATT) { X[i] = __builtin_amdgcn_exp2f(X[i]); X[i + 1] = __builtin_amdgcn_exp2f(X[i + 1]); X[i + 2] = __builtin_amdgcn_exp2f(X[i + 2]); PIN(X); SB(); } } while (0)
#define EX4(X, i) do { if constexpr (ATT) { X[i] = __builtin_amdgcn_exp2f(X[i]); X[i + 1] = __builtin_amdgcn_exp2f(X[i + 1]); X[i + 2] = __builtin_amdgcn_exp2f(X[i + 2]); X[i + 3] = __builtin_amdgcn_exp2f(X[i + 3]); PIN(X); SB(); } } while (0)
#define DFTM(Y, A, C) do { if constexpr (DFT) { Y = MFMA(A, C, Y); PIN(Y); SB(); } } while (0)

DI void mid_setup(Mid& m) {
    const int ln = m.lane, rr = ln & 31, hh = ln >> 5, kh = m.w >> 2;
    const int cslot = (rr == 16) ? 0 : (rr == 20) ? 4 : (rr < 20 || rr >= 28) ? 1 : 5;
#pragma unroll
    for (int s = 0; s < 3; ++s) { m.va[s] = (rr < 16) ? m.lds + M_KV + s * 8192 + kh * 4096 + 2048 + hh * 256 + rr * 16 : m.lds + M_CONST + cslot * 16; PIN(m.va[s]); }
#pragma unroll
    for (int p = 0; p < 4; ++p) { m.ca[p] = m.lds + M_C + ((m.w >> 1) * 32 + rr) * 128 + (((2 * p + hh) ^ ((rr >> 1) & 7)) << 4); PIN(m.ca[p]); }
    m.aa = m.lds + M_AB + (m.w & 1) * 8192 + ln * 16;
    m.ka = m.lds + M_KV + kh * 4096 + ln * 16;
    m.vwo = (ln >> 3) * 1024 + (((ln & 7) ^ (((ln >> 3) & 2) << 1)) << 4);
    m.idx = (int)0xDDDDDDDD;
    m.pollv = m.fb0 + (ln & 15) * 8;
    m.o0 = f32x16{}; m.o1 = f32x16{};
    PIN(m.aa); PIN(m.ka); PIN(m.vwo); PIN(m.idx); PIN(m.pollv); PIN(m.o0); PIN(m.o1);
    if (m.w >= 6) {
        const unsigned one2 = ((ln & 11) == 0) ? 0x3f803f80u : 0u;
        const u32x4 cv = {one2, one2, one2, one2};
        *(LAS u32x4*)(m.lds + M_CONST + (m.w - 6) * 1024 + ln * 16) = cv;
    }
}

template <int J, int U> DI void half_stage(Mid& m, Frag& F, Frag& N) {
    constexpr int NJ = U ? J + 1 : J, NU = U ^ 1;
    constexpr bool DFT = J < 16, ATT = J >= LEAD, NDFT = NJ < 16, NATT = NJ >= LEAD && NJ <= LAST, FIRST = J == LEAD && U == 0;
    constexpr int CS = (NJ % 3) * 16384, KS = NATT ? (NJ - LEAD) % 3 : 0;
    const f32x16 zero = {};
    SB();
    if constexpr (J == 0 && U == 0) { m.y0 = MFMA(F.a0, F.c0, zero); PIN(m.y0); SB(); } else DFTM(m.y0, F.a0, F.c0);
    if constexpr (NATT) { N.k = ldsfrag(m.ka + KS * 8192 + NU * 1024); SB(); }
    EX3(m.s0, 0);
    if constexpr (ATT && !FIRST) { m.o1 = pv_mfma<8>(N.v1, m.s1, m.o1, m.idx); PIN(m.o1); SB(); }
    if constexpr (NDFT) { N.a0 = ldsfrag(m.aa + CS + NU * 2048); N.c0 = ldsfrag(m.ca[2 * NU] + CS); SB(); }
    EX3(m.s0, 3);
    if constexpr (ATT) { m.s1 = MFMA(F.k, m.q1, zero); PIN(m.s1); SB(); }
    if constexpr (U) mid_dma<J, 0>(m); else mid_dma<J - 1, 3>(m);
    SB();
    EX3(m.s0, 6);
    if constexpr (J == 0 && U == 0) { m.y1 = MFMA(F.a1, F.c0, zero); PIN(m.y1); SB(); } else DFTM(m.y1, F.a1, F.c0);
    if constexpr (NATT) { N.v0 = ldsfrag(m.va[KS] + NU * 1024); N.v1 = ldsfrag(m.va[KS] + NU * 1024 + 512); SB(); }
    EX3(m.s0, 9);
    if constexpr (ATT) { m.o0 = pv_mfma<0>(F.v0, m.s0, m.o0, m.idx); PIN(m.o0); SB(); }
    EX4(m.s0, 12);
    DFTM(m.y0, F.a2, F.c1);
    if constexpr (NDFT) { N.a1 = ldsfrag(m.aa + CS + NU * 2048 + 4096); SB(); }
    if constexpr (U) mid_dma<J, 1>(m); else mid_dma<J - 1, 4>(m);
    SB();
    EX3(m.s1, 0);
    if constexpr (ATT) { m.o0 = pv_mfma<8>(F.v1, m.s0, m.o0, m.idx); PIN(m.o0); SB(); }
    if constexpr (NDFT) { N.a2 = ldsfrag(m.aa + CS + NU * 2048 + 1024); N.c1 = ldsfrag(m.ca[2 * NU + 1] + CS); SB(); }
    EX3(m.s1, 3);
    if constexpr (NATT) { m.s0 = MFMA(N.k, m.q0, zero); PIN(m.s0); SB(); }
    EX3(m.s1, 6);
    DFTM(m.y1, F.a3, F.c1);
    if constexpr (NDFT) { N.a3 = ldsfrag(m.aa + CS + NU * 2048 + 4096 + 1024); SB(); }
    EX3(m.s1, 9);
    if constexpr (ATT) { m.o1 = pv_mfma<0>(F.v0, m.s1, m.o1, m.idx); PIN(m.o1); SB(); }
    if constexpr (U) mid_dma<J, 2>(m); else mid_dma<J - 1, 5>(m);
    SB();
    EX4(m.s1, 12);
}

template <int J> DI void mid_stage(Mid& m) {
    half_stage<J, 0>(m, m.fa, m.fb);
    if constexpr (J < LAST) {
        if constexpr (J == 0) wait_vm<4>();
        else if constexpr (J == LEAD - 1 && KVLEAD == 2) wait_vm<6>();
        else wait_vm<ops_after(J - 1)>();
        if constexpr (J == KV_AT) { if (m.w == 7) poll_finish(m.pv, m.sw, m.pollv, m.epoch); }
        bar();
        if constexpr (J == LEAD - 1) asm volatile("" : "+v"(m.q0), "+v"(m.q1));
        if constexpr (J == 17) { if (threadIdx.x == 0) post_flag(m.ws, m.fo4, m.epoch); }
        if constexpr (J == KV_AT - 1) { if (m.w == 7) poll_issue(m.pv, m.sw, m.pollv); }
    }
    half_stage<J, 1>(m, m.fb, m.fa);
    if constexpr (J == 15) {
        store16c(pack8<0>(m.y0, 1.f), m.sw, m.vl, m.op);
        store16c(pack8<8>(m.y0, 1.f), m.sw, m.vl, m.op + 1024);
        store16c(pack8<0>(m.y1, 1.f), m.sw, m.vl, m.op + 2048);
        store16c(pack8<8>(m.y1, 1.f), m.sw, m.vl, m.op + 3072);
    }
}
template <int J, int E> DI void mid_stages(Mid& m) {
    if constexpr (J < E) { mid_stage<J>(m); mid_stages<J + 1, E>(m); }
}

struct Ids { int loc, mi, nj, head, qb, sp, mt, nt, tt, et; unsigned rot; };

struct ProjSrc { u32x4 sx, sr, sq; unsigned vx, vr, vq; lds_ptr xa[4], ra[4], va, wa; };
DI void proj_stage_dma(const Mid& m, const Ids& id, const ProjSrc& ps, int st, int piece) {
    const unsigned d = m.l0 + (st & 3) * P_SLOT + m.w * 1024, kr = (st + id.rot) & 7;
    if (piece == 0) dma16(d, ps.vx, ps.sx, id.mi * 131072 + kr * 128);
    if (piece == 1) dma16(d + 8192, ps.vx, ps.sx, id.mi * 131072 + kr * 128 + 65536);
    if (piece == 2) dma16(d + 16384, ps.vr, ps.sr, id.nj * 32768 + kr * 128);
    if (piece == 3) dma16(d + 20480, ps.vq, ps.sq, (kr * 4 + (m.w & 3)) * 49152 + 2048 + id.nj * 64);
}
template <int CTRL, int RMASK> DI float dpp_sum(float v) {
    return v + __int_as_float(__builtin_amdgcn_update_dpp(0, __float_as_int(v), CTRL, RMASK, 0xf, true));
}

template <int ROLE>
DI void proj(Mid& m, const Ids& id, const ProjSrc& ps) {
    const int lane = m.lane, w = m.w, r = lane & 31, h = lane >> 5, wm = w & 3, mi = id.mi, nj = id.nj;
    const unsigned l0 = m.l0;
    const lds_ptr lds = m.lds;
    char* ws = m.ws;
    constexpr int NS = 3 + ROLE;
    auto w_piece = [&](int i, unsigned wv) {
        dma16(l0 + (i < 6 ? P_W0 : P_W1 - 49152) + (8 * i + wv) * 1024, ps.vq, ps.sq, ((4 * i + (wv >> 1) + 4 * id.rot) & 31) * 49152 + (wv & 1) * 1024 + nj * 64);
    };
    const lds_ptr *xa = ps.xa, *ra = ps.ra, va = ps.va, wa = ps.wa;
    auto p1_copy = [&](int st, int q) {
        if (st <= 4) { if (q < NS) proj_stage_dma(m, id, ps, st + 3, q); }
        else if (q < 2) w_piece(2 * (st - 5) + q, w);
        SB();
    };
    const f32x16 zero = {};
    bf16x8 xf[32], rf[2], vf[2];
    f32x16 cab, cv;
    SB();
#pragma unroll
    for (int q = 0; q < NS; ++q) proj_stage_dma(m, id, ps, 2, q);
#define VFRAG(p) ldsfrag_tr(va + so + (p) * 1024, va + so + (p) * 1024 + 256)
#pragma unroll
    for (int st = 0; st < 8; ++st) {
        if (st <= 1) wait_bar<2 * NS + 1>();
        else if (st <= 5) wait_bar<2 * NS>();
        else if (st == 6) wait_bar<NS + 2>();
        else wait_bar<4>();
        if (st == 0) STAMP(0);
        STAMP1(st + 1);
        const int so = (st & 3) * P_SLOT;
        bf16x8 v0, v1;
        SB();
        if (st > 0) { cab = MFMA(xf[4 * st - 2], rf[0], cab); PIN(cab); SB(); }
        p1_copy(st, 0);
        xf[4 * st] = ldsfrag(xa[0] + so);
        const bf16x8 r0 = ldsfrag(ra[0] + so);
        if (ROLE == 0) v0 = VFRAG(0);
        SB();
        if (st > 0 && ROLE == 0) { cv = MFMA(xf[4 * st - 2], vf[0], cv); PIN(cv); SB(); }
        xf[4 * st + 1] = ldsfrag(xa[1] + so);
        const bf16x8 r1 = ldsfrag(ra[1] + so);
        if (ROLE == 0) v1 = VFRAG(1);
        SB();
        if (st > 0) { cab = MFMA(xf[4 * st - 1], rf[1], cab); PIN(cab); SB(); }
        p1_copy(st, 1);
        xf[4 * st + 2] = ldsfrag(xa[2] + so);
        rf[0] = ldsfrag(ra[2] + so);
        if (ROLE == 0) vf[0] = VFRAG(2);
        SB();
        if (st > 0 && ROLE == 0) { cv = MFMA(xf[4 * st - 1], vf[1], cv); PIN(cv); SB(); }
        xf[4 * st + 3] = ldsfrag(xa[3] + so);
        rf[1] = ldsfrag(ra[3] + so);
        if (ROLE == 0) vf[1] = VFRAG(3);
        SB();
        cab = MFMA(xf[4 * st], r0, st == 0 ? zero : cab); PIN(cab); SB();
        p1_copy(st, 2);
        if (ROLE == 0) { cv = MFMA(xf[4 * st], v0, st == 0 ? zero : cv); PIN(cv); SB(); }
        cab = MFMA(xf[4 * st + 1], r1, cab); PIN(cab); SB();
        p1_copy(st, 3);
        if (ROLE == 0) { cv = MFMA(xf[4 * st + 1], v1, cv); PIN(cv); SB(); }
    }
#undef VFRAG
    wait_bar<4>();
    STAMP(1);
    STAMP1(9);
#define WFRAG(ks) ((ks) < 24 ? ldsfrag_tr(wa + 16384 + (ks) * 2048, wa + 16384 + (ks) * 2048 + 256) : ldsfrag_tr(wa + ((ks) - 24) * 2048, wa + ((ks) - 24) * 2048 + 256))
    constexpr int PD = 3;
    bf16x8 wf[PD];
#pragma unroll
    for (int ks = 0; ks < PD; ++ks) wf[ks] = WFRAG(ks);
    SB();
    cab = MFMA(xf[30], rf[0], cab); PIN(cab); SB();
    if (ROLE == 0) { cv = MFMA(xf[30], vf[0], cv); PIN(cv); SB(); }
    cab = MFMA(xf[31], rf[1], cab); PIN(cab); SB();
    if (ROLE == 0) { cv = MFMA(xf[31], vf[1], cv); PIN(cv); SB(); }
    const int tile = mi * 4 + wm;
    f32x16 c0;
#pragma unroll
    for (int ks = 0; ks < 32; ++ks) {
        SB();
        if (ks + PD == 8) wait_bar<ROLE ? 7 : 3>();
        if (ks + PD == 16) wait_bar<ROLE ? 4 : 2>();
        SB();
        c0 = MFMA(wf[ks % PD], xf[ks], ks == 0 ? zero : c0); PIN(c0); SB();
        if (ks + PD < 32) { wf[ks % PD] = WFRAG(ks + PD); SB(); }
        const unsigned oa = WS_AB + nj * 262144 + ROLE * 131072 + tile * 2048;
        if (ks == (ROLE ? 4 : 1)) store16c(natural_order(pack8<0>(cab, ROLE ? -1.f : 1.f)), m.sw, m.vl, oa);
        if (ks == (ROLE ? 10 : 7)) store16c(natural_order(pack8<8>(cab, ROLE ? -1.f : 1.f)), m.sw, m.vl, oa + 1024);
        if (ROLE == 0) {
            const unsigned ov = WS_V + 2 * nj * 65536 + tile * 1024, vv = (r >> 4) * 65536 + h * 256 + (r & 15) * 16;
            if (ks == 13) store16c(pack8<0>(cv, 1.f), m.sw, vv, ov);
            if (ks == 16) store16c(pack8<8>(cv, 1.f), m.sw, vv, ov + 512);
        } else {
            if (ks < 4) w_piece(6 + (ks >> 1), (ks & 1) ? w : w - 4);
            if (ks >= 11 && ks < 19) {
                const int st = (ks - 11) >> 2, qr = (ks - 11) & 3;
                dma16p(l0 + M_C + st * 16384 + qr * 4096 + wm * 1024, m.vc - 131072, m.sc, m.oc + ((st + m.rot) & 15) * 128 + qr * 131072);
            }
        }
    }
#undef WFRAG
    constexpr float QS = 0.25f * 1.4426950408889634f;
    const int slot = (r & 19) | ((r & 4) << 1) | ((r & 8) >> 1);
    const unsigned oqk = (ROLE ? WS_K : WS_Q) + 2 * nj * 65536 + tile * 1024, vqk = ROLE ? h * 512 + slot * 16 : m.vl;
    u32x4 qk0 = pack8<0>(c0, ROLE ? 1.f : QS), qk1 = pack8<8>(c0, ROLE ? 1.f : QS);
    PIN(qk0); PIN(qk1);
    STAMP1(11);
    wait_bar<ROLE ? 8 : 0>();
    if (threadIdx.x == 0) post_flag(ws, WS_F + (nj * 16 + mi) * 8, m.epoch);
    STAMP(2);
    STAMP1(10);
    STAMP3(3);
    store16c(qk0, m.sw, vqk, oqk);
    store16c(qk1, m.sw, vqk, oqk + 65536);
}

extern "C" __global__ __launch_bounds__(512, 2) void k_fused(const char* x, const char* cr, const char* ci, const char* wr, const char* wi,
                                                              const char* wqkv, const char* wo, const char* gw, char* ws, char* out) {
    extern __shared__ __attribute__((aligned(16))) char smem[];
    STAMP_BEGIN();
    const lds_ptr lds = (lds_ptr)smem;
    const int lane = threadIdx.x & 63, w = __builtin_amdgcn_readfirstlane(threadIdx.x >> 6);
    {
        u64 pc;
        unsigned len;
        asm volatile("s_getpc_b64 %0\n.Lpc0:\n\ts_mov_b32 %1, .Lpc1-.Lpc0" : "=s"(pc), "=s"(len));
        const u32x4 scode = make_srd((const void*)pc, len);
        asm volatile("s_mov_b32 m0, %0\n\ts_nop 0\n\tbuffer_load_dword %1, %2, 0 offen lds" ::"s"((unsigned)(uintptr_t)lds + P_W0 + w * 256), "v"(threadIdx.x * 96), "s"(scode) : "memory");
    }
    const int g = blockIdx.x & 7, i = blockIdx.x >> 3;
    Ids id;
    id.loc = i;
    id.mi = (g & 3) * 4 + (i & 3); id.nj = (g >> 2) * 8 + (i >> 2);
    id.rot = ROT ? ((i >> 2) + 2 * (i & 3)) & 7 : 0;
    Mid m;
    m.lds = lds; m.l0 = (unsigned)(uintptr_t)lds; m.w = w; m.lane = lane; m.ws = ws;
    ProjSrc ps;
    {
        const int xrow = 8 * w + (lane >> 3);
        ps.vx = xrow * 1024 + (((lane & 7) ^ ((xrow >> 1) & 7)) << 4);
        ps.vr = ps.vx & 0x7fff;
        ps.vq = (lane >> 2) * 3072 + (lane & 3) * 16;
        ps.sx = make_srd(x, S * D * 2); ps.sr = make_srd(w < 4 ? wr : wi, D * D * 2); ps.sq = make_srd(wqkv, D * 3 * D * 2);
    }
    STAMP1(0);
    u32x4 gx, gv;
    u64 gen;
    asm volatile("global_load_dwordx4 %0, %3, off\n\tglobal_load_dwordx4 %1, %4, off\n\tglobal_load_dwordx2 %2, %5, off sc1"
                 : "=&v"(gx), "=&v"(gv), "=&v"(gen) : "v"(x + (size_t)(id.mi * 128 + id.nj * 8 + w) * 1024 + lane * 16), "v"(gw + lane * 16), "v"(ws + WS_GEN) : "memory");
#pragma unroll
    for (int st = 0; st < 2; ++st) {
        proj_stage_dma(m, id, ps, st, 0); proj_stage_dma(m, id, ps, st, 1); proj_stage_dma(m, id, ps, st, 2);
        if (w >= 4) proj_stage_dma(m, id, ps, st, 3);
        if (st == 0) bar();
    }
    SB();
    id.head = g * 4 + (i >> 3); id.qb = i & 7;
    id.sp = g >> 1; id.mt = (g & 1) * 8 + (i >> 2); id.nt = i & 3;
    id.tt = g * 4 + (i >> 3); id.et = i & 7;
    m.sc = make_srd(id.sp < 2 ? cr : ci, S * S * 2); m.sw = make_srd(ws, WS_END); m.so = make_srd(wo, D * D * 2);
    m.rw = __builtin_amdgcn_make_buffer_rsrc(ws, 0, WS_END, 0x00020000);
    m.vc = (ps.vx & 1023) + ((ps.vx & ~1023u) << 2);
    m.vl = lane * 16;
    m.rot = ROT2 ? (ROT2 * id.nt + (i >> 2)) & 15 : 0;
    m.oc = id.mt * 128 * 4096 + (id.sp & 1) * 2048;
    m.oab = WS_AB + (id.nt * 4 + (w >> 1)) * 262144 + (id.sp * 64 + (w & 1) * 2) * 1024;
    {
        const int r = lane & 31, h = lane >> 5;
#pragma unroll
        for (int p = 0; p < 4; ++p) {
            const int sw16 = ((2 * p + h) ^ ((r >> 1) & 7)) << 4;
            ps.xa[p] = lds + (32 * (w & 3) + r) * 128 + sw16;
            ps.ra[p] = lds + 16384 + (w >> 2) * 4096 + r * 128 + sw16;
            PIN(ps.xa[p]); PIN(ps.ra[p]);
        }
        const int wl = (8 * h + ((lane & 15) >> 2)) * 64 + ((lane >> 4) & 1) * 32 + (lane & 3) * 8;
        ps.va = lds + 24576 + wl; ps.wa = lds + P_W1 + (w >> 2) * 1024 + wl;
        PIN(ps.va); PIN(ps.wa); PIN(m.vc); PIN(m.vl);
        if (w < 4) asm volatile("s_waitcnt vmcnt(6)" ::: "memory"); else asm volatile("s_waitcnt vmcnt(8)" ::: "memory");
        asm volatile("" : "+v"(gx), "+v"(gv), "+v"(gen));
        m.epoch = gen + 1;
        float gate = 0.f;
#pragma unroll
        for (int q = 0; q < 4; ++q) gate += bf_lo(gx[q]) * bf_lo(gv[q]) + bf_hi(gx[q]) * bf_hi(gv[q]);
        gate = dpp_sum<0x111, 0xf>(gate); gate = dpp_sum<0x112, 0xf>(gate); gate = dpp_sum<0x114, 0xf>(gate); gate = dpp_sum<0x118, 0xf>(gate);
        gate = dpp_sum<0x142, 0xa>(gate); gate = dpp_sum<0x143, 0xc>(gate);
        gate = __builtin_amdgcn_rcpf(1.f + __expf(-gate));
        if (lane == 63) __hip_atomic_store((float*)(ws + WS_G) + id.mi * 128 + id.nj * 8 + w, gate, __ATOMIC_RELAXED, __HIP_MEMORY_SCOPE_AGENT);
    }
    if (w < 4) proj<0>(m, id, ps);
    else proj<1>(m, id, ps);
    const u64 epoch = m.epoch;
    const int r = lane & 31, h = lane >> 5, qg = w & 3, kh = w >> 2, tb = (w >> 1) & 1, eb = w & 1;
    const int head = id.head, qb = id.qb, tt = id.tt, et = id.et;
    m.okv = ((w & 2) ? WS_V : WS_K) + id.head * 65536 + (kh * 32 + (w & 1)) * 1024;
    m.owo = w * 16384 + id.et * 128;
    m.op = WS_P + id.sp * (2u << 20) + (id.mt * 4 + (w >> 1)) * 32768 + (id.nt * 4 + (w & 1) * 2) * 2048;
    m.oq = WS_Q + id.head * 65536 + (id.qb * 8 + qg * 2 + kh) * 1024;
    m.opp = WS_P + kh * (4u << 20) + (id.tt * 2 + tb) * 32768 + (id.et * 2 + eb) * 2048;
    m.og = WS_G + (id.tt * 64 + tb * 32) * 4;
    m.fb0 = WS_F + 2048 + (id.head >> 1) * 128;
    m.fc0 = WS_F + 6144 + ((id.tt >> 1) * 4 + (id.et >> 1)) * 32;
    m.fc1 = WS_F + ((id.tt & 1) * 128 + (id.tt >> 1)) * 8;
    m.fo2 = WS_F + 2048 + (id.nj * 16 + id.mi) * 8;
    m.fo4 = WS_F + 6144 + ((id.mt * 4 + id.nt) * 4 + id.sp) * 8;
    mid_setup(m);
    {
        const unsigned fa = ((id.nt * 4 + ((lane >> 3) & 3)) * 16 + (id.sp & 1) * 8 + (lane & 7)) * 8, fv = ((id.head >> 1) * 16 + (lane & 15)) * 8;
        const unsigned polla = WS_F + (lane < 32 ? fa : fv);
        if (w == 7) poll_issue(m.pv, m.sw, polla);
        wait_bar<0>();
        if (threadIdx.x == 0) post_flag(ws, m.fo2, epoch);
        STAMP(3);
        STAMP3(4);
        if (w == 7) poll_finish(m.pv, m.sw, polla, epoch);
        bar();
        STAMP3(2);
    }
#pragma unroll
    for (int st = 0; st < 3; ++st) {
        dma16c(m.l0 + M_AB + st * 16384 + w * 2048, m.vl, m.sw, m.oab + ((st + m.rot) & 15) * 4096);
        dma16c(m.l0 + M_AB + st * 16384 + w * 2048 + 1024, m.vl, m.sw, m.oab + ((st + m.rot) & 15) * 4096 + 1024);
        if (st == 0) bar();
    }
    dma16p(m.l0 + M_C + 32768 + w * 1024, m.vc, m.sc, m.oc + ((2 + m.rot) & 15) * 128);
    dma16p(m.l0 + M_C + 32768 + 8192 + w * 1024, m.vc, m.sc, m.oc + ((2 + m.rot) & 15) * 128 + 262144);
    wait_bar<6>();
    STAMP(4);
    STAMP2(0);
    m.fa.a0 = ldsfrag(m.aa); m.fa.c0 = ldsfrag(m.ca[0]); m.fa.a1 = ldsfrag(m.aa + 4096);
    m.fa.a2 = ldsfrag(m.aa + 1024); m.fa.c1 = ldsfrag(m.ca[1]); m.fa.a3 = ldsfrag(m.aa + 4096 + 1024);
    mid_stages<0, LEAD>(m);
    STAMP(5);
    mid_stages<LEAD, 16>(m);
    STAMP(6);
    mid_stages<16, LAST + 1>(m);
    m.o1 = pv_mfma<8>(m.fb.v1, m.s1, m.o1, m.idx);
    STAMP(7);

    {
        const f32x16 zero = {};
        const lds_ptr mine = lds + M_X + w * 3072 + lane * 16, other = lds + M_X + (w ^ 4) * 3072 + lane * 16;
#pragma unroll
        for (int j = 0; j < 3; ++j) *(LAS f32x4*)(mine + j * 1024) = f32x4{m.o1[4 * j], m.o1[4 * j + 1], m.o1[4 * j + 2], m.o1[4 * j + 3]};
        asm volatile("s_waitcnt lgkmcnt(0)\n\ts_barrier" ::: "memory");
        f32x16 o = m.o0;
#pragma unroll
        for (int j = 0; j < 3; ++j) {
            const f32x4 t = *(const LAS f32x4*)(other + j * 1024);
#pragma unroll
            for (int e = 0; e < 4; ++e) o[4 * j + e] += t[e];
        }
        bool ok = o[8] >= 8.271806e-25f && o[8] <= 1.2676506e30f;
#pragma unroll
        for (int e = 0; e < 8; ++e) ok = ok && (__builtin_fabsf(o[e]) <= 3.0e38f);
        if (__builtin_expect(__any(!ok), 0)) {
            const unsigned kb = WS_K + head * 65536, vb = WS_V + head * 65536, vv = h * 256 + (r & 15) * 16;
            float mx = -__builtin_inff();
            for (int t = 0; t < 64; ++t) {
                const u32x4 kf = __builtin_amdgcn_raw_buffer_load_b128(m.rw, m.vl, kb + t * 1024, 16);
                const f32x16 s = MFMA(__builtin_bit_cast(bf16x8, kf), m.q0, zero);
#pragma unroll
                for (int e = 0; e < 16; ++e) mx = __builtin_fmaxf(mx, s[e]);
            }
            mx = __builtin_fmaxf(mx, __shfl_xor(mx, 32));
            const unsigned cw = (r == 16 || r == 20) ? 0x3f803f80u : 0u;
            const u32x4 cv = {cw, cw, cw, cw};
            o = zero;
            for (int t = 0; t < 64; ++t) {
                const u32x4 kf = __builtin_amdgcn_raw_buffer_load_b128(m.rw, m.vl, kb + t * 1024, 16);
                const u32x4 v0 = __builtin_amdgcn_raw_buffer_load_b128(m.rw, vv, vb + t * 1024, 16);
                const u32x4 v1 = __builtin_amdgcn_raw_buffer_load_b128(m.rw, vv, vb + t * 1024 + 512, 16);
                f32x16 s = MFMA(__builtin_bit_cast(bf16x8, kf), m.q0, zero);
#pragma unroll
                for (int e = 0; e < 16; ++e) s[e] = __builtin_amdgcn_exp2f(s[e] - mx);
                o = pv_mfma<0>(__builtin_bit_cast(bf16x8, r < 16 ? v0 : cv), s, o, m.idx);
                o = pv_mfma<8>(__builtin_bit_cast(bf16x8, r < 16 ? v1 : cv), s, o, m.idx);
            }
        }
        store16c(pack8<0>(o, __builtin_amdgcn_rcpf(o[8])), m.sw, m.vl, WS_O + (qb * 8 + qg * 2 + kh) * 32768 + head * 1024);
    }
    if (w == 6) poll_block(m.sw, (lane & 8) ? m.fc0 + (lane & 3) * 8 : m.fc1 + (lane & 7) * 128, epoch);
    asm volatile("s_waitcnt vmcnt(0) lgkmcnt(0)\n\ts_barrier" ::: "memory");
    STAMP(8);
    STAMP3(7);
    if (threadIdx.x == 0) post_flag(ws, WS_F + 4096 + (qb * 32 + head) * 8, epoch);
    asm volatile("buffer_load_dwordx4 %0, %5, %6, %7 offen sc1\n\tbuffer_load_dwordx4 %1, %5, %6, %7 offen offset:1024 sc1\n\t"
                 "buffer_load_dwordx4 %2, %5, %6, %8 offen sc1\n\tbuffer_load_dwordx4 %3, %5, %6, %8 offen offset:1024 sc1\n\t"
                 "buffer_load_dword %4, %9, %6, %10 offen sc1"
                 : "=&v"(m.p0), "=&v"(m.p1), "=&v"(m.p2), "=&v"(m.p3), "=&v"(m.gate)
                 : "v"(m.vl), "s"(m.sw), "s"(m.opp), "s"(m.opp + (2u << 20)), "v"(r * 4), "s"(m.og) : "memory");
    if (w == 7) poll_block(m.sw, WS_F + 4096 + ((tt >> 2) * 32 + r) * 8, epoch);
    bar();
    STAMP(9);
    STAMP3(8);

#pragma unroll
    for (int c = 0; c < 4; ++c) {
        const int ks = c * 4 + (w & 3);
        dma16c(m.l0 + (kh ? o_chunk(1) : o_chunk(0)) + ks * 1024, m.vl, m.sw, WS_O + (tt * 2) * 32768 + (kh * 16 + ks) * 1024);
        dma16c(m.l0 + (kh ? o_chunk(3) : o_chunk(2)) + ks * 1024, m.vl, m.sw, WS_O + (tt * 2 + 1) * 32768 + (kh * 16 + ks) * 1024);
    }
    {
        const int q = (lane & 15) >> 2;
        const lds_ptr oa = lds + (tb ? (kh ? o_chunk(3) : o_chunk(2)) : (kh ? o_chunk(1) : o_chunk(0))) + lane * 16;
        const int wl = (4 * h + q) * 128 + ((eb * 64) ^ ((q & 2) << 5)) + ((lane >> 4) & 1) * 32 + (lane & 3) * 8;
        const lds_ptr wa0 = lds + (kh ? wo_chunk(2) : wo_chunk(0)) + wl, wa1 = lds + (kh ? wo_chunk(3) : wo_chunk(1)) + wl;
        bf16x8 wf[16];
#pragma unroll
        for (int ks = 0; ks < 16; ++ks) {
            const lds_ptr p = (ks < 8 ? wa0 : wa1) + (ks & 7) * 2048;
            wf[ks] = ldsfrag_tr(p, p + 1024);
        }
        asm volatile("s_waitcnt vmcnt(8)" : "+v"(m.p0), "+v"(m.p1), "+v"(m.p2), "+v"(m.p3), "+v"(m.gate)::"memory");
        f32x16 yv;
#pragma unroll
        for (int j = 0; j < 8; ++j) {
            const unsigned a = j < 4 ? m.p0[j & 3] : m.p1[j & 3], b = j < 4 ? m.p2[j & 3] : m.p3[j & 3];
            yv[2 * j] = m.gate * (bf_lo(a) + bf_lo(b));
            yv[2 * j + 1] = m.gate * (bf_hi(a) + bf_hi(b));
        }
        PIN(yv);
        f32x16 acc = {};
#pragma unroll
        for (int c = 0; c < 4; ++c) {
            SB();
            if (c == 0) wait_bar<6>(); else if (c == 1) wait_bar<4>(); else if (c == 2) wait_bar<2>(); else wait_bar<0>();
            bf16x8 of[4];
#pragma unroll
            for (int p = 0; p < 4; ++p) of[p] = ldsfrag(oa + (c * 4 + p) * 1024);
            SB();
#pragma unroll
            for (int p = 0; p < 4; ++p) acc = MFMA(wf[c * 4 + p], of[p], acc);
        }
        STAMP(10);
        const float og = 1.f - m.gate;
        const lds_ptr st = lds + M_X - 2048 + kh * (64 * O_PITCH) + (tb * 32 + r) * O_PITCH + (eb * 32 + 4 * h) * 4;
        asm volatile("s_waitcnt lgkmcnt(0)\n\ts_barrier" ::: "memory");
#pragma unroll
        for (int j = 0; j < 4; ++j) {
            const f32x4 v = {yv[4 * j] + og * acc[4 * j], yv[4 * j + 1] + og * acc[4 * j + 1], yv[4 * j + 2] + og * acc[4 * j + 2], yv[4 * j + 3] + og * acc[4 * j + 3]};
            *(LAS f32x4*)(st + j * 32) = v;
        }
        asm volatile("s_waitcnt lgkmcnt(0)\n\ts_barrier" ::: "memory");
        const int row = threadIdx.x >> 3, ch = threadIdx.x & 7;
        const lds_ptr rd = lds + M_X - 2048 + row * O_PITCH + ch * 32;
        const f32x4 a0 = *(const LAS f32x4*)rd, a1 = *(const LAS f32x4*)(rd + 16);
        const f32x4 b0 = *(const LAS f32x4*)(rd + 64 * O_PITCH), b1 = *(const LAS f32x4*)(rd + 64 * O_PITCH + 16);
        const u32x4 o = {pk2(a0[0] + b0[0], a0[1] + b0[1]), pk2(a0[2] + b0[2], a0[3] + b0[3]), pk2(a1[0] + b1[0], a1[1] + b1[1]), pk2(a1[2] + b1[2], a1[3] + b1[3])};
        *(u32x4*)(out + (size_t)(tt * 64 + row) * 1024 + et * 128 + ch * 16) = o;
    }
    asm volatile(".Lpc1:");
    if (blockIdx.x == 0 && w < 4) poll_block(m.sw, WS_F + 2048 + threadIdx.x * 8, epoch);
    if (blockIdx.x == 0) {
        bar();
        if (threadIdx.x == 0) post_flag(ws, WS_GEN, epoch);
    }
    STAMP_END();
}

extern "C" void kernel_launch(void* const* d_in, const int* in_sizes, int n_in, void* d_out, int out_size, void* d_ws, size_t ws_size, hipStream_t stream) {
    (void)in_sizes; (void)n_in; (void)out_size; (void)ws_size;
    (void)hipFuncSetAttribute((const void*)k_fused, hipFuncAttributeMaxDynamicSharedMemorySize, LDS_BYTES);
    k_fused<<<256, 512, LDS_BYTES, stream>>>((const char*)d_in[0], (const char*)d_in[1], (const char*)d_in[2], (const char*)d_in[3], (const char*)d_in[4],
                                             (const char*)d_in[6], (const char*)d_in[7], (const char*)d_in[8], (char*)d_ws + WSOFF, (char*)d_out);
}
```

```cpp
#define STAMPS
#define PA (-1)
#define PB (-1)
#define PK 0
#include <hip/hip_runtime.h>
#include <hip/hip_bf16.h>
#include <stdint.h>

typedef unsigned long long u64;
typedef float f32x16 __attribute__((ext_vector_type(16)));
typedef float f32x8 __attribute__((ext_vector_type(8)));
typedef float f32x4 __attribute__((ext_vector_type(4)));
typedef __bf16 bf16x8 __attribute__((ext_vector_type(8)));
typedef __bf16 bf16x16 __attribute__((ext_vector_type(16)));
typedef unsigned u32x4 __attribute__((ext_vector_type(4)));
typedef short s16x4 __attribute__((ext_vector_type(4)));
#define LAS __attribute__((address_space(3)))
typedef LAS char* lds_ptr;
#define DI __device__ __forceinline__
#define SB() __builtin_amdgcn_sched_barrier(0)
#define PIN(x) asm volatile("" : "+v"(x))
#define MFMA(a, b, c) __builtin_amdgcn_mfma_f32_32x32x16_bf16((a), (b), (c), 0, 0, 0)

constexpr int S = 2048, D = 512;
#define ROT2 0
#define WSOFF 0
#define ROT 1
#define CNT 0
#define LEAD 3
constexpr unsigned WS_Q = 0;
constexpr unsigned WS_K = 2u << 20;
constexpr unsigned WS_V = 4u << 20;
constexpr unsigned WS_AB = 6u << 20;
constexpr unsigned WS_O = 10u << 20;
constexpr unsigned WS_P = 12u << 20;
constexpr unsigned WS_G = 20u << 20;
constexpr unsigned WS_F = WS_G + 8192;
constexpr unsigned WS_GEN = WS_F + 8192;
constexpr unsigned WS_END = WS_GEN + 256;
constexpr unsigned SPIN_CAP = 1u << 20;

constexpr unsigned WS_DBG = 24u << 20;
#define STAMP_BEGIN() u64 stampv[12] = {0, 0, 0, 0, 0, 0, 0, 0, 0, 0, 0, 0}; const u64 stamp0 = __builtin_amdgcn_s_memrealtime(), stampc = __builtin_amdgcn_s_memtime(); __builtin_amdgcn_s_waitcnt(0xC07F)
#define STAMPSET 0
#define STAMPI "s_memrealtime"
#define STAMPV(v) (v)
#define STAMPX(i) do { SB(); asm volatile(STAMPI " %0\n\ts_waitcnt lgkmcnt(0)" : "=s"(stampv[i]) :: "memory"); SB(); } while (0)
#define STAMP(i) do { if (STAMPSET == 0) STAMPX(i); } while (0)
#define STAMP1(i) do { if (STAMPSET == 1) STAMPX(i); } while (0)
#define STAMP2(i) do { if (STAMPSET == 2) STAMPX(i); } while (0)
#define STAMP3(i) do { if (STAMPSET == 3) STAMPX(i); } while (0)
#define STAMPT 0
#define STAMP_END() do { const u64 te = __builtin_amdgcn_s_memrealtime(); const u64 ta = PA < 0 ? stamp0 : stampv[PA < 0 ? 0 : PA], tb = PB < 0 ? te : stampv[PB < 0 ? 0 : PB]; \
    const u64 dl = (tb - ta) * PK; if (threadIdx.x < 64) while (__builtin_amdgcn_s_memrealtime() - te < dl) __builtin_amdgcn_s_sleep(1); (void)stampc; } while (0)

DI u32x4 make_srd(const void* p, unsigned bytes) {
    const u64 a = (u64)p;
    u32x4 r = {(unsigned)a, (unsigned)(a >> 32) & 0xffffu, bytes, 0x00020000u};
    return r;
}
DI void dma16(unsigned lds, unsigned voff, u32x4 srd, unsigned soff) {
    asm volatile("s_mov_b32 m0, %0\n\ts_nop 0\n\tbuffer_load_dwordx4 %1, %2, %3 offen lds" ::"s"(lds), "v"(voff), "s"(srd), "s"(soff) : "memory");
}
DI void dma16n(unsigned lds, unsigned voff, u32x4 srd, unsigned soff) {
    asm volatile("s_mov_b32 m0, %0\n\ts_nop 0\n\tbuffer_load_dwordx4 %1, %2, %3 offen nt lds" ::"s"(lds), "v"(voff), "s"(srd), "s"(soff) : "memory");
}
DI void dma16p(unsigned lds, unsigned voff, u32x4 srd, unsigned soff) { if (CNT) dma16n(lds, voff, srd, soff); else dma16(lds, voff, srd, soff); }
DI void dma16c(unsigned lds, unsigned voff, u32x4 srd, unsigned soff) {
    asm volatile("s_mov_b32 m0, %0\n\ts_nop 0\n\tbuffer_load_dwordx4 %1, %2, %3 offen sc1 lds" ::"s"(lds), "v"(voff), "s"(srd), "s"(soff) : "memory");
}
DI void store16c(u32x4 v, u32x4 srd, unsigned voff, unsigned soff) {
    asm volatile("buffer_store_dwordx4 %0, %1, %2, %3 offen sc1\n\ts_nop 1" ::"v"(v), "v"(voff), "s"(srd), "s"(soff) : "memory");
}
DI void post_flag(char* ws, unsigned off, u64 epoch) { __hip_atomic_store((u64*)(ws + off), epoch, __ATOMIC_RELAXED, __HIP_MEMORY_SCOPE_AGENT); }
DI void poll_issue(u64& v, u32x4 srd, unsigned voff) { asm volatile("buffer_load_dwordx2 %0, %1, %2, 0 offen sc1" : "=v"(v) : "v"(voff), "s"(srd) : "memory"); }
DI void poll_finish(u64 v, u32x4 srd, unsigned voff, u64 epoch) {
    asm volatile("" : "+v"(v));
    unsigned spins = 0;
    while (!__all(v == epoch)) {
        if (++spins > SPIN_CAP) break;
        __builtin_amdgcn_s_sleep(1);
        asm volatile("buffer_load_dwordx2 %0, %1, %2, 0 offen sc1\n\ts_waitcnt vmcnt(0)" : "=&v"(v) : "v"(voff), "s"(srd) : "memory");
    }
}
DI void poll_block(u32x4 srd, unsigned voff, u64 epoch) {
    u64 v;
    asm volatile("buffer_load_dwordx2 %0, %1, %2, 0 offen sc1\n\ts_waitcnt vmcnt(0)" : "=&v"(v) : "v"(voff), "s"(srd) : "memory");
    poll_finish(v, srd, voff, epoch);
}
DI bf16x8 ldsfrag(lds_ptr p) { return *(const LAS bf16x8*)p; }
DI bf16x8 ldsfrag_tr(lds_ptr p0, lds_ptr p1) {
    const s16x4 lo = __builtin_amdgcn_ds_read_tr16_b64_v4i16((LAS s16x4*)p0);
    const s16x4 hi = __builtin_amdgcn_ds_read_tr16_b64_v4i16((LAS s16x4*)p1);
    return __builtin_bit_cast(bf16x8, __builtin_shufflevector(lo, hi, 0, 1, 2, 3, 4, 5, 6, 7));
}
DI unsigned pk2(float lo, float hi) {
    typedef __bf16 bf16x2 __attribute__((ext_vector_type(2)));
    typedef float f32x2 __attribute__((ext_vector_type(2)));
    const f32x2 v = {lo, hi};
    return __builtin_bit_cast(unsigned, __builtin_convertvector(v, bf16x2));
}
template <int B> DI u32x4 pack8(const f32x16& v, float sc) {
    u32x4 r = {pk2(v[B] * sc, v[B + 1] * sc), pk2(v[B + 2] * sc, v[B + 3] * sc), pk2(v[B + 4] * sc, v[B + 5] * sc), pk2(v[B + 6] * sc, v[B + 7] * sc)};
    return r;
}
DI u32x4 natural_order(u32x4 p) {
    const auto s0 = __builtin_amdgcn_permlane32_swap(p[0], p[2], false, false);
    const auto s1 = __builtin_amdgcn_permlane32_swap(p[1], p[3], false, false);
    u32x4 r = {s0[0], s1[0], s0[1], s1[1]};
    return r;
}
DI float bf_lo(unsigned u) { return __uint_as_float(u << 16); }
DI float bf_hi(unsigned u) { return __uint_as_float(u & 0xffff0000u); }
template <int B> DI f32x16 pv_mfma(bf16x8 vfrag, const f32x16& p, f32x16 o, int idx) {
    const f32x8 h = __builtin_shufflevector(p, p, B, B + 1, B + 2, B + 3, B + 4, B + 5, B + 6, B + 7);
    return __builtin_amdgcn_smfmac_f32_32x32x32_bf16(vfrag, __builtin_bit_cast(bf16x16, h), o, idx, 0, 0);
}
template <int N> DI void wait_vm() { asm volatile("s_waitcnt vmcnt(%0) lgkmcnt(0)" ::"i"(N) : "memory"); }
DI void bar() { asm volatile("s_barrier" ::: "memory"); }
template <int N> DI void wait_bar() { wait_vm<N>(); bar(); }

constexpr int P_SLOT = 28672, P_W0 = 114688, P_W1 = 98304;
constexpr int M_C = 0, M_AB = 49152, M_KV = 98304, M_CONST = M_KV + 3 * 8192, M_X = 131072;
constexpr int LDS_BYTES = 163840, O_PITCH = 272;
constexpr int wo_chunk(int c) { return c == 0 ? 16384 : c == 1 ? 65536 : c == 2 ? 32768 : 81920; }
constexpr int o_chunk(int c) { return c == 0 ? 0 : c == 1 ? 49152 : c == 2 ? 98304 : 114688; }
#define KVLEAD 2
constexpr int KV_AT = LEAD - KVLEAD, LAST = LEAD + 15;
constexpr int ops_after(int j) {
    int n = 0;
    if (j <= 14) n += 4;
    if (j == KV_AT) n += 5;
    if (j >= LEAD && j + 3 - LEAD < 16) n += 1;
    if (j == 15) n += 4;
    return n;
}
static_assert(KV_AT >= 1 && LEAD <= 12 && LAST > 17 && (KVLEAD == 2 || KVLEAD == 3), "phase 2 schedule");

struct Frag { bf16x8 k, v0, v1, a0, a1, c0, a2, a3, c1; };

struct Mid {
    lds_ptr lds, ka, va[3], aa, ca[4];
    unsigned l0, vc, vl, vwo, oc, oab, okv, owo, w, rot;
    u32x4 sc, sw, so;
    __amdgpu_buffer_rsrc_t rw;
    char* ws;
    unsigned op, fo2, fo4, oq, opp, og, fb0, fc0, fc1, pollv;
    u64 epoch, pv;
    int lane, idx;
    bf16x8 q0, q1;
    f32x16 o0, o1, y0, y1, s0, s1;
    Frag fa, fb;
    u32x4 p0, p1, p2, p3;
    float gate;
    u64* stampv;
};

template <int J, int P> DI void mid_dma(Mid& m) {
    constexpr int SL = J % 3;
    if constexpr (J < 0 || P > 4) {
    } else if constexpr (J + 3 < 16) {
        const unsigned ks = (J + 3 + m.rot) & 15;
        if constexpr (P == 1) dma16p(m.l0 + M_C + SL * 16384 + m.w * 1024, m.vc, m.sc, m.oc + ks * 128);
        if constexpr (P == 2) dma16p(m.l0 + M_C + SL * 16384 + 8192 + m.w * 1024, m.vc, m.sc, m.oc + ks * 128 + 262144);
        if constexpr (P == 3) dma16c(m.l0 + M_AB + SL * 16384 + m.w * 2048, m.vl, m.sw, m.oab + ks * 4096);
        if constexpr (P == 4) dma16c(m.l0 + M_AB + SL * 16384 + m.w * 2048 + 1024, m.vl, m.sw, m.oab + ks * 4096 + 1024);
    } else if constexpr (J <= 14 && P >= 1) {
        constexpr int c = (J - 13) * 2 + (P - 1) / 2, hf = (P - 1) & 1;
        dma16n(m.l0 + wo_chunk(c) + m.w * 2048 + hf * 1024, m.vwo, m.so, m.owo + c * 131072 + hf * 8192);
    }
    if constexpr (P == 0) {
        if constexpr (J == KV_AT) {
            asm volatile("buffer_load_dwordx4 %0, %2, %3, %4 offen sc1\n\tbuffer_load_dwordx4 %1, %2, %3, %5 offen sc1"
                         : "=&v"(m.q0), "=&v"(m.q1) : "v"(m.vl), "s"(m.sw), "s"(m.oq), "s"(m.oq ^ 1024) : "memory");
            dma16c(m.l0 + M_KV + m.w * 1024, m.vl, m.sw, m.okv);
        }
        if constexpr (J >= LEAD && J + 3 - LEAD < 16) dma16c(m.l0 + M_KV + ((J - LEAD) % 3) * 8192 + m.w * 1024, m.vl, m.sw, m.okv + (J + 3 - LEAD) * 2048);
    }
    if constexpr (P == 5 && J == KV_AT) {
        dma16c(m.l0 + M_KV + 8192 + m.w * 1024, m.vl, m.sw, m.okv + 2048);
        dma16c(m.l0 + M_KV + 16384 + m.w * 1024, m.vl, m.sw, m.okv + 4096);
    }
}

#define EX3(X, i) do { if constexpr (ATT) { X[i] = __builtin_amdgcn_exp2f(X[i]); X[i + 1] = __builtin_amdgcn_exp2f(X[i + 1]); X[i + 2] = __builtin_amdgcn_exp2f(X[i + 2]); PIN(X); SB(); } } while (0)
#define EX4(X, i) do { if constexpr (ATT) { X[i] = __builtin_amdgcn_exp2f(X[i]); X[i + 1] = __builtin_amdgcn_exp2f(X[i + 1]); X[i + 2] = __builtin_amdgcn_exp2f(X[i + 2]); X[i + 3] = __builtin_amdgcn_exp2f(X[i + 3]); PIN(X); SB(); } } while (0)
#define DFTM(Y, A, C) do { if constexpr (DFT) { Y = MFMA(A, C, Y); PIN(Y); SB(); } } while (0)

DI void mid_setup(Mid& m) {
    const int ln = m.lane, rr = ln & 31, hh = ln >> 5, kh = m.w >> 2;
    const int cslot = (rr == 16) ? 0 : (rr == 20) ? 4 : (rr < 20 || rr >= 28) ? 1 : 5;
#pragma unroll
    for (int s = 0; s < 3; ++s) { m.va[s] = (rr < 16) ? m.lds + M_KV + s * 8192 + kh * 4096 + 2048 + hh * 256 + rr * 16 : m.lds + M_CONST + cslot * 16; PIN(m.va[s]); }
#pragma unroll
    for (int p = 0; p < 4; ++p) { m.ca[p] = m.lds + M_C + ((m.w >> 1) * 32 + rr) * 128 + (((2 * p + hh) ^ ((rr >> 1) & 7)) << 4); PIN(m.ca[p]); }
    m.aa = m.lds + M_AB + (m.w & 1) * 8192 + ln * 16;
    m.ka = m.lds + M_KV + kh * 4096 + ln * 16;
    m.vwo = (ln >> 3) * 1024 + (((ln & 7) ^ (((ln >> 3) & 2) << 1)) << 4);
    m.idx = (int)0xDDDDDDDD;
    m.pollv = m.fb0 + (ln & 15) * 8;
    m.o0 = f32x16{}; m.o1 = f32x16{};
    PIN(m.aa); PIN(m.ka); PIN(m.vwo); PIN(m.idx); PIN(m.pollv); PIN(m.o0); PIN(m.o1);
    if (m.w >= 6) {
        const unsigned one2 = ((ln & 11) == 0) ? 0x3f803f80u : 0u;
        const u32x4 cv = {one2, one2, one2, one2};
        *(LAS u32x4*)(m.lds + M_CONST + (m.w - 6) * 1024 + ln * 16) = cv;
    }
}

template <int J, int U> DI void half_stage(Mid& m, Frag& F, Frag& N) {
    constexpr int NJ = U ? J + 1 : J, NU = U ^ 1;
    constexpr bool DFT = J < 16, ATT = J >= LEAD, NDFT = NJ < 16, NATT = NJ >= LEAD && NJ <= LAST, FIRST = J == LEAD && U == 0;
    constexpr int CS = (NJ % 3) * 16384, KS = NATT ? (NJ - LEAD) % 3 : 0;
    const f32x16 zero = {};
    SB();
    if constexpr (J == 0 && U == 0) { m.y0 = MFMA(F.a0, F.c0, zero); PIN(m.y0); SB(); } else DFTM(m.y0, F.a0, F.c0);
    if constexpr (NATT) { N.k = ldsfrag(m.ka + KS * 8192 + NU * 1024); SB(); }
    EX3(m.s0, 0);
    if constexpr (ATT && !FIRST) { m.o1 = pv_mfma<8>(N.v1, m.s1, m.o1, m.idx); PIN(m.o1); SB(); }
    if constexpr (NDFT) { N.a0 = ldsfrag(m.aa + CS + NU * 2048); N.c0 = ldsfrag(m.ca[2 * NU] + CS); SB(); }
    EX3(m.s0, 3);
    if constexpr (ATT) { m.s1 = MFMA(F.k, m.q1, zero); PIN(m.s1); SB(); }
    if constexpr (U) mid_dma<J, 0>(m); else mid_dma<J - 1, 3>(m);
    SB();
    EX3(m.s0, 6);
    if constexpr (J == 0 && U == 0) { m.y1 = MFMA(F.a1, F.c0, zero); PIN(m.y1); SB(); } else DFTM(m.y1, F.a1, F.c0);
    if constexpr (NATT) { N.v0 = ldsfrag(m.va[KS] + NU * 1024); N.v1 = ldsfrag(m.va[KS] + NU * 1024 + 512); SB(); }
    EX3(m.s0, 9);
    if constexpr (ATT) { m.o0 = pv_mfma<0>(F.v0, m.s0, m.o0, m.idx); PIN(m.o0); SB(); }
    EX4(m.s0, 12);
    DFTM(m.y0, F.a2, F.c1);
    if constexpr (NDFT) { N.a1 = ldsfrag(m.aa + CS + NU * 2048 + 4096); SB(); }
    if constexpr (U) mid_dma<J, 1>(m); else mid_dma<J - 1, 4>(m);
    SB();
    EX3(m.s1, 0);
    if constexpr (ATT) { m.o0 = pv_mfma<8>(F.v1, m.s0, m.o0, m.idx); PIN(m.o0); SB(); }
    if constexpr (NDFT) { N.a2 = ldsfrag(m.aa + CS + NU * 2048 + 1024); N.c1 = ldsfrag(m.ca[2 * NU + 1] + CS); SB(); }
    EX3(m.s1, 3);
    if constexpr (NATT) { m.s0 = MFMA(N.k, m.q0, zero); PIN(m.s0); SB(); }
    EX3(m.s1, 6);
    DFTM(m.y1, F.a3, F.c1);
    if constexpr (NDFT) { N.a3 = ldsfrag(m.aa + CS + NU * 2048 + 4096 + 1024); SB(); }
    EX3(m.s1, 9);
    if constexpr (ATT) { m.o1 = pv_mfma<0>(F.v0, m.s1, m.o1, m.idx); PIN(m.o1); SB(); }
    if constexpr (U) mid_dma<J, 2>(m); else mid_dma<J - 1, 5>(m);
    SB();
    EX4(m.s1, 12);
}

template <int J> DI void mid_stage(Mid& m) {
    half_stage<J, 0>(m, m.fa, m.fb);
    if constexpr (J < LAST) {
        if constexpr (J == 0) wait_vm<4>();
        else if constexpr (J == LEAD - 1 && KVLEAD == 2) wait_vm<6>();
        else wait_vm<ops_after(J - 1)>();
        if constexpr (J == KV_AT) { u64* stampv = m.stampv; STAMP3(5); }
        if constexpr (J == KV_AT) { if (m.w == 7) poll_finish(m.pv, m.sw, m.pollv, m.epoch); }
        if constexpr (J == KV_AT) { u64* stampv = m.stampv; STAMP3(6); }
        bar();
        { u64* stampv = m.stampv; if constexpr (J < 11) STAMP2(J + 1); }
        if constexpr (J == LEAD - 1) asm volatile("" : "+v"(m.q0), "+v"(m.q1));
        if constexpr (J == 17) { if (threadIdx.x == 0) post_flag(m.ws, m.fo4, m.epoch); }
        if constexpr (J == KV_AT - 1) { if (m.w == 7) poll_issue(m.pv, m.sw, m.pollv); }
    }
    half_stage<J, 1>(m, m.fb, m.fa);
    if constexpr (J == 15) {
        store16c(pack8<0>(m.y0, 1.f), m.sw, m.vl, m.op);
        store16c(pack8<8>(m.y0, 1.f), m.sw, m.vl, m.op + 1024);
        store16c(pack8<0>(m.y1, 1.f), m.sw, m.vl, m.op + 2048);
        store16c(pack8<8>(m.y1, 1.f), m.sw, m.vl, m.op + 3072);
    }
}
template <int J, int E> DI void mid_stages(Mid& m) {
    if constexpr (J < E) { mid_stage<J>(m); mid_stages<J + 1, E>(m); }
}

struct Ids { int loc, mi, nj, head, qb, sp, mt, nt, tt, et; unsigned rot; };

struct ProjSrc { u32x4 sx, sr, sq; unsigned vx, vr, vq; lds_ptr xa[4], ra[4], va, wa; };
DI void proj_stage_dma(const Mid& m, const Ids& id, const ProjSrc& ps, int st, int piece) {
    const unsigned d = m.l0 + (st & 3) * P_SLOT + m.w * 1024, kr = (st + id.rot) & 7;
    if (piece == 0) dma16(d, ps.vx, ps.sx, id.mi * 131072 + kr * 128);
    if (piece == 1) dma16(d + 8192, ps.vx, ps.sx, id.mi * 131072 + kr * 128 + 65536);
    if (piece == 2) dma16(d + 16384, ps.vr, ps.sr, id.nj * 32768 + kr * 128);
    if (piece == 3) dma16(d + 20480, ps.vq, ps.sq, (kr * 4 + (m.w & 3)) * 49152 + 2048 + id.nj * 64);
}
template <int CTRL, int RMASK> DI float dpp_sum(float v) {
    return v + __int_as_float(__builtin_amdgcn_update_dpp(0, __float_as_int(v), CTRL, RMASK, 0xf, true));
}

template <int ROLE>
DI void proj(Mid& m, const Ids& id, const ProjSrc& ps) {
    u64* stampv = m.stampv;
    const int lane = m.lane, w = m.w, r = lane & 31, h = lane >> 5, wm = w & 3, mi = id.mi, nj = id.nj;
    const unsigned l0 = m.l0;
    const lds_ptr lds = m.lds;
    char* ws = m.ws;
    constexpr int NS = 3 + ROLE;
    auto w_piece = [&](int i, unsigned wv) {
        dma16(l0 + (i < 6 ? P_W0 : P_W1 - 49152) + (8 * i + wv) * 1024, ps.vq, ps.sq, ((4 * i + (wv >> 1) + 4 * id.rot) & 31) * 49152 + (wv & 1) * 1024 + nj * 64);
    };
    const lds_ptr *xa = ps.xa, *ra = ps.ra, va = ps.va, wa = ps.wa;
    auto p1_copy = [&](int st, int q) {
        if (st <= 4) { if (q < NS) proj_stage_dma(m, id, ps, st + 3, q); }
        else if (q < 2) w_piece(2 * (st - 5) + q, w);
        SB();
    };
    const f32x16 zero = {};
    bf16x8 xf[32], rf[2], vf[2];
    f32x16 cab, cv;
    SB();
#pragma unroll
    for (int q = 0; q < NS; ++q) proj_stage_dma(m, id, ps, 2, q);
#define VFRAG(p) ldsfrag_tr(va + so + (p) * 1024, va + so + (p) * 1024 + 256)
#pragma unroll
    for (int st = 0; st < 8; ++st) {
        if (st <= 1) wait_bar<2 * NS + 1>();
        else if (st <= 5) wait_bar<2 * NS>();
        else if (st == 6) wait_bar<NS + 2>();
        else wait_bar<4>();
        if (st == 0) STAMP(0);
        STAMP1(st + 1);
        const int so = (st & 3) * P_SLOT;
        bf16x8 v0, v1;
        SB();
        if (st > 0) { cab = MFMA(xf[4 * st - 2], rf[0], cab); PIN(cab); SB(); }
        p1_copy(st, 0);
        xf[4 * st] = ldsfrag(xa[0] + so);
        const bf16x8 r0 = ldsfrag(ra[0] + so);
        if (ROLE == 0) v0 = VFRAG(0);
        SB();
        if (st > 0 && ROLE == 0) { cv = MFMA(xf[4 * st - 2], vf[0], cv); PIN(cv); SB(); }
        xf[4 * st + 1] = ldsfrag(xa[1] + so);
        const bf16x8 r1 = ldsfrag(ra[1] + so);
        if (ROLE == 0) v1 = VFRAG(1);
        SB();
        if (st > 0) { cab = MFMA(xf[4 * st - 1], rf[1], cab); PIN(cab); SB(); }
        p1_copy(st, 1);
        xf[4 * st + 2] = ldsfrag(xa[2] + so);
        rf[0] = ldsfrag(ra[2] + so);
        if (ROLE == 0) vf[0] = VFRAG(2);
        SB();
        if (st > 0 && ROLE == 0) { cv = MFMA(xf[4 * st - 1], vf[1], cv); PIN(cv); SB(); }
        xf[4 * st + 3] = ldsfrag(xa[3] + so);
        rf[1] = ldsfrag(ra[3] + so);
        if (ROLE == 0) vf[1] = VFRAG(3);
        SB();
        cab = MFMA(xf[4 * st], r0, st == 0 ? zero : cab); PIN(cab); SB();
        p1_copy(st, 2);
        if (ROLE == 0) { cv = MFMA(xf[4 * st], v0, st == 0 ? zero : cv); PIN(cv); SB(); }
        cab = MFMA(xf[4 * st + 1], r1, cab); PIN(cab); SB();
        p1_copy(st, 3);
        if (ROLE == 0) { cv = MFMA(xf[4 * st + 1], v1, cv); PIN(cv); SB(); }
    }
#undef VFRAG
    wait_bar<4>();
    STAMP(1);
    STAMP1(9);
#define WFRAG(ks) ((ks) < 24 ? ldsfrag_tr(wa + 16384 + (ks) * 2048, wa + 16384 + (ks) * 2048 + 256) : ldsfrag_tr(wa + ((ks) - 24) * 2048, wa + ((ks) - 24) * 2048 + 256))
    constexpr int PD = 3;
    bf16x8 wf[PD];
#pragma unroll
    for (int ks = 0; ks < PD; ++ks) wf[ks] = WFRAG(ks);
    SB();
    cab = MFMA(xf[30], rf[0], cab); PIN(cab); SB();
    if (ROLE == 0) { cv = MFMA(xf[30], vf[0], cv); PIN(cv); SB(); }
    cab = MFMA(xf[31], rf[1], cab); PIN(cab); SB();
    if (ROLE == 0) { cv = MFMA(xf[31], vf[1], cv); PIN(cv); SB(); }
    const int tile = mi * 4 + wm;
    f32x16 c0;
#pragma unroll
    for (int ks = 0; ks < 32; ++ks) {
        SB();
        if (ks + PD == 8) wait_bar<ROLE ? 7 : 3>();
        if (ks + PD == 16) wait_bar<ROLE ? 4 : 2>();
        SB();
        c0 = MFMA(wf[ks % PD], xf[ks], ks == 0 ? zero : c0); PIN(c0); SB();
        if (ks + PD < 32) { wf[ks % PD] = WFRAG(ks + PD); SB(); }
        const unsigned oa = WS_AB + nj * 262144 + ROLE * 131072 + tile * 2048;
        if (ks == (ROLE ? 4 : 1)) store16c(natural_order(pack8<0>(cab, ROLE ? -1.f : 1.f)), m.sw, m.vl, oa);
        if (ks == (ROLE ? 10 : 7)) store16c(natural_order(pack8<8>(cab, ROLE ? -1.f : 1.f)), m.sw, m.vl, oa + 1024);
        if (ROLE == 0) {
            const unsigned ov = WS_V + 2 * nj * 65536 + tile * 1024, vv = (r >> 4) * 65536 + h * 256 + (r & 15) * 16;
            if (ks == 13) store16c(pack8<0>(cv, 1.f), m.sw, vv, ov);
            if (ks == 16) store16c(pack8<8>(cv, 1.f), m.sw, vv, ov + 512);
        } else {
            if (ks < 4) w_piece(6 + (ks >> 1), (ks & 1) ? w : w - 4);
            if (ks >= 11 && ks < 19) {
                const int st = (ks - 11) >> 2, qr = (ks - 11) & 3;
                dma16p(l0 + M_C + st * 16384 + qr * 4096 + wm * 1024, m.vc - 131072, m.sc, m.oc + ((st + m.rot) & 15) * 128 + qr * 131072);
            }
        }
    }
#undef WFRAG
    constexpr float QS = 0.25f * 1.4426950408889634f;
    const int slot = (r & 19) | ((r & 4) << 1) | ((r & 8) >> 1);
    const unsigned oqk = (ROLE ? WS_K : WS_Q) + 2 * nj * 65536 + tile * 1024, vqk = ROLE ? h * 512 + slot * 16 : m.vl;
    u32x4 qk0 = pack8<0>(c0, ROLE ? 1.f : QS), qk1 = pack8<8>(c0, ROLE ? 1.f : QS);
    PIN(qk0); PIN(qk1);
    STAMP1(11);
    wait_bar<ROLE ? 8 : 0>();
    if (threadIdx.x == 0) post_flag(ws, WS_F + (nj * 16 + mi) * 8, m.epoch);
    STAMP(2);
    STAMP1(10);
    STAMP3(3);
    store16c(qk0, m.sw, vqk, oqk);
    store16c(qk1, m.sw, vqk, oqk + 65536);
}

extern "C" __global__ __launch_bounds__(512, 2) void k_fused(const char* x, const char* cr, const char* ci, const char* wr, const char* wi,
                                                              const char* wqkv, const char* wo, const char* gw, char* ws, char* out) {
    extern __shared__ __attribute__((aligned(16))) char smem[];
    STAMP_BEGIN();
    const lds_ptr lds = (lds_ptr)smem;
    const int lane = threadIdx.x & 63, w = __builtin_amdgcn_readfirstlane(threadIdx.x >> 6);
    {
        u64 pc;
        unsigned len;
        asm volatile("s_getpc_b64 %0\n.Lpc0:\n\ts_mov_b32 %1, .Lpc1-.Lpc0" : "=s"(pc), "=s"(len));
        const u32x4 scode = make_srd((const void*)pc, len);
        asm volatile("s_mov_b32 m0, %0\n\ts_nop 0\n\tbuffer_load_dword %1, %2, 0 offen lds" ::"s"((unsigned)(uintptr_t)lds + P_W0 + w * 256), "v"(threadIdx.x * 96), "s"(scode) : "memory");
    }
    const int g = blockIdx.x & 7, i = blockIdx.x >> 3;
    Ids id;
    id.loc = i;
    id.mi = (g & 3) * 4 + (i & 3); id.nj = (g >> 2) * 8 + (i >> 2);
    id.rot = ROT ? ((i >> 2) + 2 * (i & 3)) & 7 : 0;
    Mid m;
    m.lds = lds; m.l0 = (unsigned)(uintptr_t)lds; m.w = w; m.lane = lane; m.ws = ws;
    m.stampv = stampv;
    ProjSrc ps;
    {
        const int xrow = 8 * w + (lane >> 3);
        ps.vx = xrow * 1024 + (((lane & 7) ^ ((xrow >> 1) & 7)) << 4);
        ps.vr = ps.vx & 0x7fff;
        ps.vq = (lane >> 2) * 3072 + (lane & 3) * 16;
        ps.sx = make_srd(x, S * D * 2); ps.sr = make_srd(w < 4 ? wr : wi, D * D * 2); ps.sq = make_srd(wqkv, D * 3 * D * 2);
    }
    STAMP1(0);
    u32x4 gx, gv;
    u64 gen;
    asm volatile("global_load_dwordx4 %0, %3, off\n\tglobal_load_dwordx4 %1, %4, off\n\tglobal_load_dwordx2 %2, %5, off sc1"
                 : "=&v"(gx), "=&v"(gv), "=&v"(gen) : "v"(x + (size_t)(id.mi * 128 + id.nj * 8 + w) * 1024 + lane * 16), "v"(gw + lane * 16), "v"(ws + WS_GEN) : "memory");
#pragma unroll
    for (int st = 0; st < 2; ++st) {
        proj_stage_dma(m, id, ps, st, 0); proj_stage_dma(m, id, ps, st, 1); proj_stage_dma(m, id, ps, st, 2);
        if (w >= 4) proj_stage_dma(m, id, ps, st, 3);
        if (st == 0) bar();
    }
    SB();
    id.head = g * 4 + (i >> 3); id.qb = i & 7;
    id.sp = g >> 1; id.mt = (g & 1) * 8 + (i >> 2); id.nt = i & 3;
    id.tt = g * 4 + (i >> 3); id.et = i & 7;
    m.sc = make_srd(id.sp < 2 ? cr : ci, S * S * 2); m.sw = make_srd(ws, WS_END); m.so = make_srd(wo, D * D * 2);
    m.rw = __builtin_amdgcn_make_buffer_rsrc(ws, 0, WS_END, 0x00020000);
    m.vc = (ps.vx & 1023) + ((ps.vx & ~1023u) << 2);
    m.vl = lane * 16;
    m.rot = ROT2 ? (ROT2 * id.nt + (i >> 2)) & 15 : 0;
    m.oc = id.mt * 128 * 4096 + (id.sp & 1) * 2048;
    m.oab = WS_AB + (id.nt * 4 + (w >> 1)) * 262144 + (id.sp * 64 + (w & 1) * 2) * 1024;
    {
        const int r = lane & 31, h = lane >> 5;
#pragma unroll
        for (int p = 0; p < 4; ++p) {
            const int sw16 = ((2 * p + h) ^ ((r >> 1) & 7)) << 4;
            ps.xa[p] = lds + (32 * (w & 3) + r) * 128 + sw16;
            ps.ra[p] = lds + 16384 + (w >> 2) * 4096 + r * 128 + sw16;
            PIN(ps.xa[p]); PIN(ps.ra[p]);
        }
        const int wl = (8 * h + ((lane & 15) >> 2)) * 64 + ((lane >> 4) & 1) * 32 + (lane & 3) * 8;
        ps.va = lds + 24576 + wl; ps.wa = lds + P_W1 + (w >> 2) * 1024 + wl;
        PIN(ps.va); PIN(ps.wa); PIN(m.vc); PIN(m.vl);
        if (w < 4) asm volatile("s_waitcnt vmcnt(6)" ::: "memory"); else asm volatile("s_waitcnt vmcnt(8)" ::: "memory");
        asm volatile("" : "+v"(gx), "+v"(gv), "+v"(gen));
        m.epoch = gen + 1;
        float gate = 0.f;
#pragma unroll
        for (int q = 0; q < 4; ++q) gate += bf_lo(gx[q]) * bf_lo(gv[q]) + bf_hi(gx[q]) * bf_hi(gv[q]);
        gate = dpp_sum<0x111, 0xf>(gate); gate = dpp_sum<0x112, 0xf>(gate); gate = dpp_sum<0x114, 0xf>(gate); gate = dpp_sum<0x118, 0xf>(gate);
        gate = dpp_sum<0x142, 0xa>(gate); gate = dpp_sum<0x143, 0xc>(gate);
        gate = __builtin_amdgcn_rcpf(1.f + __expf(-gate));
        if (lane == 63) __hip_atomic_store((float*)(ws + WS_G) + id.mi * 128 + id.nj * 8 + w, gate, __ATOMIC_RELAXED, __HIP_MEMORY_SCOPE_AGENT);
    }
    if (w < 4) proj<0>(m, id, ps);
    else proj<1>(m, id, ps);
    const u64 epoch = m.epoch;
    const int r = lane & 31, h = lane >> 5, qg = w & 3, kh = w >> 2, tb = (w >> 1) & 1, eb = w & 1;
    const int head = id.head, qb = id.qb, tt = id.tt, et = id.et;
    m.okv = ((w & 2) ? WS_V : WS_K) + id.head * 65536 + (kh * 32 + (w & 1)) * 1024;
    m.owo = w * 16384 + id.et * 128;
    m.op = WS_P + id.sp * (2u << 20) + (id.mt * 4 + (w >> 1)) * 32768 + (id.nt * 4 + (w & 1) * 2) * 2048;
    m.oq = WS_Q + id.head * 65536 + (id.qb * 8 + qg * 2 + kh) * 1024;
    m.opp = WS_P + kh * (4u << 20) + (id.tt * 2 + tb) * 32768 + (id.et * 2 + eb) * 2048;
    m.og = WS_G + (id.tt * 64 + tb * 32) * 4;
    m.fb0 = WS_F + 2048 + (id.head >> 1) * 128;
    m.fc0 = WS_F + 6144 + ((id.tt >> 1) * 4 + (id.et >> 1)) * 32;
    m.fc1 = WS_F + ((id.tt & 1) * 128 + (id.tt >> 1)) * 8;
    m.fo2 = WS_F + 2048 + (id.nj * 16 + id.mi) * 8;
    m.fo4 = WS_F + 6144 + ((id.mt * 4 + id.nt) * 4 + id.sp) * 8;
    mid_setup(m);
    {
        const unsigned fa = ((id.nt * 4 + ((lane >> 3) & 3)) * 16 + (id.sp & 1) * 8 + (lane & 7)) * 8, fv = ((id.head >> 1) * 16 + (lane & 15)) * 8;
        const unsigned polla = WS_F + (lane < 32 ? fa : fv);
        if (w == 7) poll_issue(m.pv, m.sw, polla);
        wait_bar<0>();
        if (threadIdx.x == 0) post_flag(ws, m.fo2, epoch);
        STAMP(3);
        STAMP3(4);
        if (w == 7) poll_finish(m.pv, m.sw, polla, epoch);
        bar();
        STAMP3(2);
    }
#pragma unroll
    for (int st = 0; st < 3; ++st) {
        dma16c(m.l0 + M_AB + st * 16384 + w * 2048, m.vl, m.sw, m.oab + ((st + m.rot) & 15) * 4096);
        dma16c(m.l0 + M_AB + st * 16384 + w * 2048 + 1024, m.vl, m.sw, m.oab + ((st + m.rot) & 15) * 4096 + 1024);
        if (st == 0) bar();
    }
    dma16p(m.l0 + M_C + 32768 + w * 1024, m.vc, m.sc, m.oc + ((2 + m.rot) & 15) * 128);
    dma16p(m.l0 + M_C + 32768 + 8192 + w * 1024, m.vc, m.sc, m.oc + ((2 + m.rot) & 15) * 128 + 262144);
    wait_bar<6>();
    STAMP(4);
    STAMP2(0);
    m.fa.a0 = ldsfrag(m.aa); m.fa.c0 = ldsfrag(m.ca[0]); m.fa.a1 = ldsfrag(m.aa + 4096);
    m.fa.a2 = ldsfrag(m.aa + 1024); m.fa.c1 = ldsfrag(m.ca[1]); m.fa.a3 = ldsfrag(m.aa + 4096 + 1024);
    mid_stages<0, LEAD>(m);
    STAMP(5);
    mid_stages<LEAD, 16>(m);
    STAMP(6);
    mid_stages<16, LAST + 1>(m);
    m.o1 = pv_mfma<8>(m.fb.v1, m.s1, m.o1, m.idx);
    STAMP(7);

    {
        const f32x16 zero = {};
        const lds_ptr mine = lds + M_X + w * 3072 + lane * 16, other = lds + M_X + (w ^ 4) * 3072 + lane * 16;
#pragma unroll
        for (int j = 0; j < 3; ++j) *(LAS f32x4*)(mine + j * 1024) = f32x4{m.o1[4 * j], m.o1[4 * j + 1], m.o1[4 * j + 2], m.o1[4 * j + 3]};
        asm volatile("s_waitcnt lgkmcnt(0)\n\ts_barrier" ::: "memory");
        f32x16 o = m.o0;
#pragma unroll
        for (int j = 0; j < 3; ++j) {
            const f32x4 t = *(const LAS f32x4*)(other + j * 1024);
#pragma unroll
            for (int e = 0; e < 4; ++e) o[4 * j + e] += t[e];
        }
        bool ok = o[8] >= 8.271806e-25f && o[8] <= 1.2676506e30f;
#pragma unroll
        for (int e = 0; e < 8; ++e) ok = ok && (__builtin_fabsf(o[e]) <= 3.0e38f);
        if (__builtin_expect(__any(!ok), 0)) {
            const unsigned kb = WS_K + head * 65536, vb = WS_V + head * 65536, vv = h * 256 + (r & 15) * 16;
            float mx = -__builtin_inff();
            for (int t = 0; t < 64; ++t) {
                const u32x4 kf = __builtin_amdgcn_raw_buffer_load_b128(m.rw, m.vl, kb + t * 1024, 16);
                const f32x16 s = MFMA(__builtin_bit_cast(bf16x8, kf), m.q0, zero);
#pragma unroll
                for (int e = 0; e < 16; ++e) mx = __builtin_fmaxf(mx, s[e]);
            }
            mx = __builtin_fmaxf(mx, __shfl_xor(mx, 32));
            const unsigned cw = (r == 16 || r == 20) ? 0x3f803f80u : 0u;
            const u32x4 cv = {cw, cw, cw, cw};
            o = zero;
            for (int t = 0; t < 64; ++t) {
                const u32x4 kf = __builtin_amdgcn_raw_buffer_load_b128(m.rw, m.vl, kb + t * 1024, 16);
                const u32x4 v0 = __builtin_amdgcn_raw_buffer_load_b128(m.rw, vv, vb + t * 1024, 16);
                const u32x4 v1 = __builtin_amdgcn_raw_buffer_load_b128(m.rw, vv, vb + t * 1024 + 512, 16);
                f32x16 s = MFMA(__builtin_bit_cast(bf16x8, kf), m.q0, zero);
#pragma unroll
                for (int e = 0; e < 16; ++e) s[e] = __builtin_amdgcn_exp2f(s[e] - mx);
                o = pv_mfma<0>(__builtin_bit_cast(bf16x8, r < 16 ? v0 : cv), s, o, m.idx);
                o = pv_mfma<8>(__builtin_bit_cast(bf16x8, r < 16 ? v1 : cv), s, o, m.idx);
            }
        }
        store16c(pack8<0>(o, __builtin_amdgcn_rcpf(o[8])), m.sw, m.vl, WS_O + (qb * 8 + qg * 2 + kh) * 32768 + head * 1024);
    }
    if (w == 6) poll_block(m.sw, (lane & 8) ? m.fc0 + (lane & 3) * 8 : m.fc1 + (lane & 7) * 128, epoch);
    asm volatile("s_waitcnt vmcnt(0) lgkmcnt(0)\n\ts_barrier" ::: "memory");
    STAMP(8);
    STAMP3(7);
    if (threadIdx.x == 0) post_flag(ws, WS_F + 4096 + (qb * 32 + head) * 8, epoch);
    asm volatile("buffer_load_dwordx4 %0, %5, %6, %7 offen sc1\n\tbuffer_load_dwordx4 %1, %5, %6, %7 offen offset:1024 sc1\n\t"
                 "buffer_load_dwordx4 %2, %5, %6, %8 offen sc1\n\tbuffer_load_dwordx4 %3, %5, %6, %8 offen offset:1024 sc1\n\t"
                 "buffer_load_dword %4, %9, %6, %10 offen sc1"
                 : "=&v"(m.p0), "=&v"(m.p1), "=&v"(m.p2), "=&v"(m.p3), "=&v"(m.gate)
                 : "v"(m.vl), "s"(m.sw), "s"(m.opp), "s"(m.opp + (2u << 20)), "v"(r * 4), "s"(m.og) : "memory");
    if (w == 7) poll_block(m.sw, WS_F + 4096 + ((tt >> 2) * 32 + r) * 8, epoch);
    bar();
    STAMP(9);
    STAMP3(8);

#pragma unroll
    for (int c = 0; c < 4; ++c) {
        const int ks = c * 4 + (w & 3);
        dma16c(m.l0 + (kh ? o_chunk(1) : o_chunk(0)) + ks * 1024, m.vl, m.sw, WS_O + (tt * 2) * 32768 + (kh * 16 + ks) * 1024);
        dma16c(m.l0 + (kh ? o_chunk(3) : o_chunk(2)) + ks * 1024, m.vl, m.sw, WS_O + (tt * 2 + 1) * 32768 + (kh * 16 + ks) * 1024);
    }
    {
        const int q = (lane & 15) >> 2;
        const lds_ptr oa = lds + (tb ? (kh ? o_chunk(3) : o_chunk(2)) : (kh ? o_chunk(1) : o_chunk(0))) + lane * 16;
        const int wl = (4 * h + q) * 128 + ((eb * 64) ^ ((q & 2) << 5)) + ((lane >> 4) & 1) * 32 + (lane & 3) * 8;
        const lds_ptr wa0 = lds + (kh ? wo_chunk(2) : wo_chunk(0)) + wl, wa1 = lds + (kh ? wo_chunk(3) : wo_chunk(1)) + wl;
        bf16x8 wf[16];
#pragma unroll
        for (int ks = 0; ks < 16; ++ks) {
            const lds_ptr p = (ks < 8 ? wa0 : wa1) + (ks & 7) * 2048;
            wf[ks] = ldsfrag_tr(p, p + 1024);
        }
        asm volatile("s_waitcnt vmcnt(8)" : "+v"(m.p0), "+v"(m.p1), "+v"(m.p2), "+v"(m.p3), "+v"(m.gate)::"memory");
        f32x16 yv;
#pragma unroll
        for (int j = 0; j < 8; ++j) {
            const unsigned a = j < 4 ? m.p0[j & 3] : m.p1[j & 3], b = j < 4 ? m.p2[j & 3] : m.p3[j & 3];
            yv[2 * j] = m.gate * (bf_lo(a) + bf_lo(b));
            yv[2 * j + 1] = m.gate * (bf_hi(a) + bf_hi(b));
        }
        PIN(yv);
        f32x16 acc = {};
#pragma unroll
        for (int c = 0; c < 4; ++c) {
            SB();
            if (c == 0) wait_bar<6>(); else if (c == 1) wait_bar<4>(); else if (c == 2) wait_bar<2>(); else wait_bar<0>();
            bf16x8 of[4];
#pragma unroll
            for (int p = 0; p < 4; ++p) of[p] = ldsfrag(oa + (c * 4 + p) * 1024);
            SB();
#pragma unroll
            for (int p = 0; p < 4; ++p) acc = MFMA(wf[c * 4 + p], of[p], acc);
        }
        STAMP(10);
        const float og = 1.f - m.gate;
        const lds_ptr st = lds + M_X - 2048 + kh * (64 * O_PITCH) + (tb * 32 + r) * O_PITCH + (eb * 32 + 4 * h) * 4;
        asm volatile("s_waitcnt lgkmcnt(0)\n\ts_barrier" ::: "memory");
#pragma unroll
        for (int j = 0; j < 4; ++j) {
            const f32x4 v = {yv[4 * j] + og * acc[4 * j], yv[4 * j + 1] + og * acc[4 * j + 1], yv[4 * j + 2] + og * acc[4 * j + 2], yv[4 * j + 3] + og * acc[4 * j + 3]};
            *(LAS f32x4*)(st + j * 32) = v;
        }
        asm volatile("s_waitcnt lgkmcnt(0)\n\ts_barrier" ::: "memory");
        const int row = threadIdx.x >> 3, ch = threadIdx.x & 7;
        const lds_ptr rd = lds + M_X - 2048 + row * O_PITCH + ch * 32;
        const f32x4 a0 = *(const LAS f32x4*)rd, a1 = *(const LAS f32x4*)(rd + 16);
        const f32x4 b0 = *(const LAS f32x4*)(rd + 64 * O_PITCH), b1 = *(const LAS f32x4*)(rd + 64 * O_PITCH + 16);
        const u32x4 o = {pk2(a0[0] + b0[0], a0[1] + b0[1]), pk2(a0[2] + b0[2], a0[3] + b0[3]), pk2(a1[0] + b1[0], a1[1] + b1[1]), pk2(a1[2] + b1[2], a1[3] + b1[3])};
        *(u32x4*)(out + (size_t)(tt * 64 + row) * 1024 + et * 128 + ch * 16) = o;
    }
    asm volatile(".Lpc1:");
    if (blockIdx.x == 0 && w < 4) poll_block(m.sw, WS_F + 2048 + threadIdx.x * 8, epoch);
    if (blockIdx.x == 0) {
        bar();
        if (threadIdx.x == 0) post_flag(ws, WS_GEN, epoch);
    }
    STAMP_END();
}

extern "C" void kernel_launch(void* const* d_in, const int* in_sizes, int n_in, void* d_out, int out_size, void* d_ws, size_t ws_size, hipStream_t stream) {
    (void)in_sizes; (void)n_in; (void)out_size; (void)ws_size;
    (void)hipFuncSetAttribute((const void*)k_fused, hipFuncAttributeMaxDynamicSharedMemorySize, LDS_BYTES);
    k_fused<<<256, 512, LDS_BYTES, stream>>>((const char*)d_in[0], (const char*)d_in[1], (const char*)d_in[2], (const char*)d_in[3], (const char*)d_in[4],
                                             (const char*)d_in[6], (const char*)d_in[7], (const char*)d_in[8], (char*)d_ws + WSOFF, (char*)d_out);
}
```

```cpp
#include <hip/hip_runtime.h>
#include <hip/hip_bf16.h>
#include <stdint.h>

typedef unsigned long long u64;
typedef float f32x16 __attribute__((ext_vector_type(16)));
typedef float f32x8 __attribute__((ext_vector_type(8)));
typedef float f32x4 __attribute__((ext_vector_type(4)));
typedef __bf16 bf16x8 __attribute__((ext_vector_type(8)));
typedef __bf16 bf16x16 __attribute__((ext_vector_type(16)));
typedef unsigned u32x4 __attribute__((ext_vector_type(4)));
typedef short s16x4 __attribute__((ext_vector_type(4)));
#define LAS __attribute__((address_space(3)))
typedef LAS char* lds_ptr;
#define DI __device__ __forceinline__
#define SB() __builtin_amdgcn_sched_barrier(0)
#define PIN(x) asm volatile("" : "+v"(x))
#define MFMA(a, b, c) __builtin_amdgcn_mfma_f32_32x32x16_bf16((a), (b), (c), 0, 0, 0)

constexpr int S = 2048, D = 512;
#define ROT2 0
#define WSOFF 0
#define ROT 1
#define CNT 0
#define LEAD 3
constexpr unsigned WS_Q = 0;
constexpr unsigned WS_K = 2u << 20;
constexpr unsigned WS_V = 4u << 20;
constexpr unsigned WS_AB = 6u << 20;
constexpr unsigned WS_O = 10u << 20;
constexpr unsigned WS_P = 12u << 20;
constexpr unsigned WS_G = 20u << 20;
constexpr unsigned WS_F = WS_G + 8192;
constexpr unsigned WS_GEN = WS_F + 8192;
constexpr unsigned WS_END = WS_GEN + 256;
constexpr unsigned SPIN_CAP = 1u << 20;

#define STAMP_BEGIN()
#define STAMP(i)
#define STAMP1(i)
#define STAMP2(i)
#define STAMP3(i)
#define STAMP_END()

DI u32x4 make_srd(const void* p, unsigned bytes) {
    const u64 a = (u64)p;
    u32x4 r = {(unsigned)a, (unsigned)(a >> 32) & 0xffffu, bytes, 0x00020000u};
    return r;
}
DI void dma16(unsigned lds, unsigned voff, u32x4 srd, unsigned soff) {
    asm volatile("s_mov_b32 m0, %0\n\ts_nop 0\n\tbuffer_load_dwordx4 %1, %2, %3 offen lds" ::"s"(lds), "v"(voff), "s"(srd), "s"(soff) : "memory");
}
DI void dma16n(unsigned lds, unsigned voff, u32x4 srd, unsigned soff) {
    asm volatile("s_mov_b32 m0, %0\n\ts_nop 0\n\tbuffer_load_dwordx4 %1, %2, %3 offen nt lds" ::"s"(lds), "v"(voff), "s"(srd), "s"(soff) : "memory");
}
DI void dma16p(unsigned lds, unsigned voff, u32x4 srd, unsigned soff) { if (CNT) dma16n(lds, voff, srd, soff); else dma16(lds, voff, srd, soff); }
DI void dma16c(unsigned lds, unsigned voff, u32x4 srd, unsigned soff) {
    asm volatile("s_mov_b32 m0, %0\n\ts_nop 0\n\tbuffer_load_dwordx4 %1, %2, %3 offen sc1 lds" ::"s"(lds), "v"(voff), "s"(srd), "s"(soff) : "memory");
}
DI void store16c(u32x4 v, u32x4 srd, unsigned voff, unsigned soff) {
    asm volatile("buffer_store_dwordx4 %0, %1, %2, %3 offen sc1\n\ts_nop 1" ::"v"(v), "v"(voff), "s"(srd), "s"(soff) : "memory");
}
DI void post_flag(char* ws, unsigned off, u64 epoch) { __hip_atomic_store((u64*)(ws + off), epoch, __ATOMIC_RELAXED, __HIP_MEMORY_SCOPE_AGENT); }
DI void poll_issue(u64& v, u32x4 srd, unsigned voff) { asm volatile("buffer_load_dwordx2 %0, %1, %2, 0 offen sc1" : "=v"(v) : "v"(voff), "s"(srd) : "memory"); }
DI void poll_finish(u64 v, u32x4 srd, unsigned voff, u64 epoch) {
    asm volatile("" : "+v"(v));
    unsigned spins = 0;
    while (!__all(v == epoch)) {
        if (++spins > SPIN_CAP) break;
        __builtin_amdgcn_s_sleep(1);
        asm volatile("buffer_load_dwordx2 %0, %1, %2, 0 offen sc1\n\ts_waitcnt vmcnt(0)" : "=&v"(v) : "v"(voff), "s"(srd) : "memory");
    }
}
DI void poll_block(u32x4 srd, unsigned voff, u64 epoch) {
    u64 v;
    asm volatile("buffer_load_dwordx2 %0, %1, %2, 0 offen sc1\n\ts_waitcnt vmcnt(0)" : "=&v"(v) : "v"(voff), "s"(srd) : "memory");
    poll_finish(v, srd, voff, epoch);
}
DI bf16x8 ldsfrag(lds_ptr p) { return *(const LAS bf16x8*)p; }
DI bf16x8 ldsfrag_tr(lds_ptr p0, lds_ptr p1) {
    const s16x4 lo = __builtin_amdgcn_ds_read_tr16_b64_v4i16((LAS s16x4*)p0);
    const s16x4 hi = __builtin_amdgcn_ds_read_tr16_b64_v4i16((LAS s16x4*)p1);
    return __builtin_bit_cast(bf16x8, __builtin_shufflevector(lo, hi, 0, 1, 2, 3, 4, 5, 6, 7));
}
DI unsigned pk2(float lo, float hi) {
    typedef __bf16 bf16x2 __attribute__((ext_vector_type(2)));
    typedef float f32x2 __attribute__((ext_vector_type(2)));
    const f32x2 v = {lo, hi};
    return __builtin_bit_cast(unsigned, __builtin_convertvector(v, bf16x2));
}
template <int B> DI u32x4 pack8(const f32x16& v, float sc) {
    u32x4 r = {pk2(v[B] * sc, v[B + 1] * sc), pk2(v[B + 2] * sc, v[B + 3] * sc), pk2(v[B + 4] * sc, v[B + 5] * sc), pk2(v[B + 6] * sc, v[B + 7] * sc)};
    return r;
}
DI u32x4 natural_order(u32x4 p) {
    const auto s0 = __builtin_amdgcn_permlane32_swap(p[0], p[2], false, false);
    const auto s1 = __builtin_amdgcn_permlane32_swap(p[1], p[3], false, false);
    u32x4 r = {s0[0], s1[0], s0[1], s1[1]};
    return r;
}
DI float bf_lo(unsigned u) { return __uint_as_float(u << 16); }
DI float bf_hi(unsigned u) { return __uint_as_float(u & 0xffff0000u); }
template <int B> DI f32x16 pv_mfma(bf16x8 vfrag, const f32x16& p, f32x16 o, int idx) {
    const f32x8 h = __builtin_shufflevector(p, p, B, B + 1, B + 2, B + 3, B + 4, B + 5, B + 6, B + 7);
    return __builtin_amdgcn_smfmac_f32_32x32x32_bf16(vfrag, __builtin_bit_cast(bf16x16, h), o, idx, 0, 0);
}
template <int N> DI void wait_vm() { asm volatile("s_waitcnt vmcnt(%0) lgkmcnt(0)" ::"i"(N) : "memory"); }
DI void bar() { asm volatile("s_barrier" ::: "memory"); }
template <int N> DI void wait_bar() { wait_vm<N>(); bar(); }

constexpr int P_SLOT = 28672, P_W0 = 114688, P_W1 = 98304;
constexpr int M_C = 0, M_AB = 49152, M_KV = 98304, M_CONST = M_KV + 3 * 8192, M_X = 131072;
constexpr int LDS_BYTES = 163840, O_PITCH = 272;
constexpr int wo_chunk(int c) { return c == 0 ? 16384 : c == 1 ? 65536 : c == 2 ? 32768 : 81920; }
constexpr int o_chunk(int c) { return c == 0 ? 0 : c == 1 ? 49152 : c == 2 ? 98304 : 114688; }
#define KVLEAD 2
constexpr int KV_AT = LEAD - KVLEAD, LAST = LEAD + 15;
constexpr int ops_after(int j) {
    int n = 0;
    if (j <= 14) n += 4;
    if (j == KV_AT) n += 5;
    if (j >= LEAD && j + 3 - LEAD < 16) n += 1;
    if (j == 15) n += 4;
    return n;
}
static_assert(KV_AT >= 1 && LEAD <= 12 && LAST > 17 && (KVLEAD == 2 || KVLEAD == 3), "phase 2 schedule");

struct Frag { bf16x8 k, v0, v1, a0, a1, c0, a2, a3, c1; };

struct Mid {
    lds_ptr lds, ka, va[3], aa, ca[4];
    unsigned l0, vc, vl, vwo, oc, oab, okv, owo, w, rot;
    u32x4 sc, sw, so;
    __amdgpu_buffer_rsrc_t rw;
    char* ws;
    unsigned op, fo2, fo4, oq, opp, og, fb0, fc0, fc1, pollv;
    u64 epoch, pv;
    int lane, idx;
    bf16x8 q0, q1;
    f32x16 o0, o1, y0, y1, s0, s1;
    Frag fa, fb;
    u32x4 p0, p1, p2, p3;
    float gate;
};

template <int J, int P> DI void mid_dma(Mid& m) {
    constexpr int SL = J % 3;
    if constexpr (J < 0 || P > 4) {
    } else if constexpr (J + 3 < 16) {
        const unsigned ks = (J + 3 + m.rot) & 15;
        if constexpr (P == 1) dma16p(m.l0 + M_C + SL * 16384 + m.w * 1024, m.vc, m.sc, m.oc + ks * 128);
        if constexpr (P == 2) dma16p(m.l0 + M_C + SL * 16384 + 8192 + m.w * 1024, m.vc, m.sc, m.oc + ks * 128 + 262144);
        if constexpr (P == 3) dma16c(m.l0 + M_AB + SL * 16384 + m.w * 2048, m.vl, m.sw, m.oab + ks * 4096);
        if constexpr (P == 4) dma16c(m.l0 + M_AB + SL * 16384 + m.w * 2048 + 1024, m.vl, m.sw, m.oab + ks * 4096 + 1024);
    } else if constexpr (J <= 14 && P >= 1) {
        constexpr int c = (J - 13) * 2 + (P - 1) / 2, hf = (P - 1) & 1;
        dma16n(m.l0 + wo_chunk(c) + m.w * 2048 + hf * 1024, m.vwo, m.so, m.owo + c * 131072 + hf * 8192);
    }
    if constexpr (P == 0) {
        if constexpr (J == KV_AT) {
            asm volatile("buffer_load_dwordx4 %0, %2, %3, %4 offen sc1\n\tbuffer_load_dwordx4 %1, %2, %3, %5 offen sc1"
                         : "=&v"(m.q0), "=&v"(m.q1) : "v"(m.vl), "s"(m.sw), "s"(m.oq), "s"(m.oq ^ 1024) : "memory");
            dma16c(m.l0 + M_KV + m.w * 1024, m.vl, m.sw, m.okv);
        }
        if constexpr (J >= LEAD && J + 3 - LEAD < 16) dma16c(m.l0 + M_KV + ((J - LEAD) % 3) * 8192 + m.w * 1024, m.vl, m.sw, m.okv + (J + 3 - LEAD) * 2048);
    }
    if constexpr (P == 5 && J == KV_AT) {
        dma16c(m.l0 + M_KV + 8192 + m.w * 1024, m.vl, m.sw, m.okv + 2048);
        dma16c(m.l0 + M_KV + 16384 + m.w * 1024, m.vl, m.sw, m.okv + 4096);
    }
}

#define EX3(X, i) do { if constexpr (ATT) { X[i] = __builtin_amdgcn_exp2f(X[i]); X[i + 1] = __builtin_amdgcn_exp2f(X[i + 1]); X[i + 2] = __builtin_amdgcn_exp2f(X[i + 2]); PIN(X); SB(); } } while (0)
#define EX4(X, i) do { if constexpr (ATT) { X[i] = __builtin_amdgcn_exp2f(X[i]); X[i + 1] = __builtin_amdgcn_exp2f(X[i + 1]); X[i + 2] = __builtin_amdgcn_exp2f(X[i + 2]); X[i + 3] = __builtin_amdgcn_exp2f(X[i + 3]); PIN(X); SB(); } } while (0)
#define DFTM(Y, A, C) do { if constexpr (DFT) { Y = MFMA(A, C, Y); PIN(Y); SB(); } } while (0)

DI void mid_setup(Mid& m) {
    const int ln = m.lane, rr = ln & 31, hh = ln >> 5, kh = m.w >> 2;
    const int cslot = (rr == 16) ? 0 : (rr == 20) ? 4 : (rr < 20 || rr >= 28) ? 1 : 5;
#pragma unroll
    for (int s = 0; s < 3; ++s) { m.va[s] = (rr < 16) ? m.lds + M_KV + s * 8192 + kh * 4096 + 2048 + hh * 256 + rr * 16 : m.lds + M_CONST + cslot * 16; PIN(m.va[s]); }
#pragma unroll
    for (int p = 0; p < 4; ++p) { m.ca[p] = m.lds + M_C + ((m.w >> 1) * 32 + rr) * 128 + (((2 * p + hh) ^ ((rr >> 1) & 7)) << 4); PIN(m.ca[p]); }
    m.aa = m.lds + M_AB + (m.w & 1) * 8192 + ln * 16;
    m.ka = m.lds + M_KV + kh * 4096 + ln * 16;
    m.vwo = (ln >> 3) * 1024 + (((ln & 7) ^ (((ln >> 3) & 2) << 1)) << 4);
    m.idx = (int)0xDDDDDDDD;
    m.pollv = m.fb0 + (ln & 15) * 8;
    m.o0 = f32x16{}; m.o1 = f32x16{};
    PIN(m.aa); PIN(m.ka); PIN(m.vwo); PIN(m.idx); PIN(m.pollv); PIN(m.o0); PIN(m.o1);
    if (m.w >= 6) {
        const unsigned one2 = ((ln & 11) == 0) ? 0x3f803f80u : 0u;
        const u32x4 cv = {one2, one2, one2, one2};
        *(LAS u32x4*)(m.lds + M_CONST + (m.w - 6) * 1024 + ln * 16) = cv;
    }
}

template <int J, int U> DI void half_stage(Mid& m, Frag& F, Frag& N) {
    constexpr int NJ = U ? J + 1 : J, NU = U ^ 1;
    constexpr bool DFT = J < 16, ATT = J >= LEAD, NDFT = NJ < 16, NATT = NJ >= LEAD && NJ <= LAST, FIRST = J == LEAD && U == 0;
    constexpr int CS = (NJ % 3) * 16384, KS = NATT ? (NJ - LEAD) % 3 : 0;
    const f32x16 zero = {};
    SB();
    if constexpr (J == 0 && U == 0) { m.y0 = MFMA(F.a0, F.c0, zero); PIN(m.y0); SB(); } else DFTM(m.y0, F.a0, F.c0);
    if constexpr (NATT) { N.k = ldsfrag(m.ka + KS * 8192 + NU * 1024); SB(); }
    EX3(m.s0, 0);
    if constexpr (ATT && !FIRST) { m.o1 = pv_mfma<8>(N.v1, m.s1, m.o1, m.idx); PIN(m.o1); SB(); }
    if constexpr (NDFT) { N.a0 = ldsfrag(m.aa + CS + NU * 2048); N.c0 = ldsfrag(m.ca[2 * NU] + CS); SB(); }
    EX3(m.s0, 3);
    if constexpr (ATT) { m.s1 = MFMA(F.k, m.q1, zero); PIN(m.s1); SB(); }
    if constexpr (U) mid_dma<J, 0>(m); else mid_dma<J - 1, 3>(m);
    SB();
    EX3(m.s0, 6);
    if constexpr (J == 0 && U == 0) { m.y1 = MFMA(F.a1, F.c0, zero); PIN(m.y1); SB(); } else DFTM(m.y1, F.a1, F.c0);
    if constexpr (NATT) { N.v0 = ldsfrag(m.va[KS] + NU * 1024); N.v1 = ldsfrag(m.va[KS] + NU * 1024 + 512); SB(); }
    EX3(m.s0, 9);
    if constexpr (ATT) { m.o0 = pv_mfma<0>(F.v0, m.s0, m.o0, m.idx); PIN(m.o0); SB(); }
    EX4(m.s0, 12);
    DFTM(m.y0, F.a2, F.c1);
    if constexpr (NDFT) { N.a1 = ldsfrag(m.aa + CS + NU * 2048 + 4096); SB(); }
    if constexpr (U) mid_dma<J, 1>(m); else mid_dma<J - 1, 4>(m);
    SB();
    EX3(m.s1, 0);
    if constexpr (ATT) { m.o0 = pv_mfma<8>(F.v1, m.s0, m.o0, m.idx); PIN(m.o0); SB(); }
    if constexpr (NDFT) { N.a2 = ldsfrag(m.aa + CS + NU * 2048 + 1024); N.c1 = ldsfrag(m.ca[2 * NU + 1] + CS); SB(); }
    EX3(m.s1, 3);
    if constexpr (NATT) { m.s0 = MFMA(N.k, m.q0, zero); PIN(m.s0); SB(); }
    EX3(m.s1, 6);
    DFTM(m.y1, F.a3, F.c1);
    if constexpr (NDFT) { N.a3 = ldsfrag(m.aa + CS + NU * 2048 + 4096 + 1024); SB(); }
    EX3(m.s1, 9);
    if constexpr (ATT) { m.o1 = pv_mfma<0>(F.v0, m.s1, m.o1, m.idx); PIN(m.o1); SB(); }
    if constexpr (U) mid_dma<J, 2>(m); else mid_dma<J - 1, 5>(m);
    SB();
    EX4(m.s1, 12);
}

template <int J> DI void mid_stage(Mid& m) {
    half_stage<J, 0>(m, m.fa, m.fb);
    if constexpr (J < LAST) {
        if constexpr (J == 0) wait_vm<4>();
        else if constexpr (J == LEAD - 1 && KVLEAD == 2) wait_vm<6>();
        else wait_vm<ops_after(J - 1)>();
        if constexpr (J == KV_AT) { if (m.w == 7) poll_finish(m.pv, m.sw, m.pollv, m.epoch); }
        bar();
        if constexpr (J == LEAD - 1) asm volatile("" : "+v"(m.q0), "+v"(m.q1));
        if constexpr (J == 17) { if (threadIdx.x == 0) post_flag(m.ws, m.fo4, m.epoch); }
        if constexpr (J == KV_AT - 1) { if (m.w == 7) poll_issue(m.pv, m.sw, m.pollv); }
    }
    half_stage<J, 1>(m, m.fb, m.fa);
    if constexpr (J == 15) {
        store16c(pack8<0>(m.y0, 1.f), m.sw, m.vl, m.op);
        store16c(pack8<8>(m.y0, 1.f), m.sw, m.vl, m.op + 1024);
        store16c(pack8<0>(m.y1, 1.f), m.sw, m.vl, m.op + 2048);
        store16c(pack8<8>(m.y1, 1.f), m.sw, m.vl, m.op + 3072);
    }
}
template <int J, int E> DI void mid_stages(Mid& m) {
    if constexpr (J < E) { mid_stage<J>(m); mid_stages<J + 1, E>(m); }
}

struct Ids { int loc, mi, nj, head, qb, sp, mt, nt, tt, et; unsigned rot; };

struct ProjSrc { u32x4 sx, sr, sq; unsigned vx, vr, vq; lds_ptr xa[4], ra[4], va, wa; };
DI void proj_stage_dma(const Mid& m, const Ids& id, const ProjSrc& ps, int st, int piece) {
    const unsigned d = m.l0 + (st & 3) * P_SLOT + m.w * 1024, kr = (st + id.rot) & 7;
    if (piece == 0) dma16(d, ps.vx, ps.sx, id.mi * 131072 + kr * 128);
    if (piece == 1) dma16(d + 8192, ps.vx, ps.sx, id.mi * 131072 + kr * 128 + 65536);
    if (piece == 2) dma16(d + 16384, ps.vr, ps.sr, id.nj * 32768 + kr * 128);
    if (piece == 3) dma16(d + 20480, ps.vq, ps.sq, (kr * 4 + (m.w & 3)) * 49152 + 2048 + id.nj * 64);
}
template <int CTRL, int RMASK> DI float dpp_sum(float v) {
    return v + __int_as_float(__builtin_amdgcn_update_dpp(0, __float_as_int(v), CTRL, RMASK, 0xf, true));
}

template <int ROLE>
DI void proj(Mid& m, const Ids& id, const ProjSrc& ps) {
    const int lane = m.lane, w = m.w, r = lane & 31, h = lane >> 5, wm = w & 3, mi = id.mi, nj = id.nj;
    const unsigned l0 = m.l0;
    const lds_ptr lds = m.lds;
    char* ws = m.ws;
    constexpr int NS = 3 + ROLE;
    auto w_piece = [&](int i, unsigned wv) {
        dma16(l0 + (i < 6 ? P_W0 : P_W1 - 49152) + (8 * i + wv) * 1024, ps.vq, ps.sq, ((4 * i + (wv >> 1) + 4 * id.rot) & 31) * 49152 + (wv & 1) * 1024 + nj * 64);
    };
    const lds_ptr *xa = ps.xa, *ra = ps.ra, va = ps.va, wa = ps.wa;
    auto p1_copy = [&](int st, int q) {
        if (st <= 4) { if (q < NS) proj_stage_dma(m, id, ps, st + 3, q); }
        else if (q < 2) w_piece(2 * (st - 5) + q, w);
        SB();
    };
    const f32x16 zero = {};
    bf16x8 xf[32], rf[2], vf[2];
    f32x16 cab, cv;
    SB();
#pragma unroll
    for (int q = 0; q < NS; ++q) proj_stage_dma(m, id, ps, 2, q);
#define VFRAG(p) ldsfrag_tr(va + so + (p) * 1024, va + so + (p) * 1024 + 256)
#pragma unroll
    for (int st = 0; st < 8; ++st) {
        if (st <= 1) wait_bar<2 * NS + 1>();
        else if (st <= 5) wait_bar<2 * NS>();
        else if (st == 6) wait_bar<NS + 2>();
        else wait_bar<4>();
        if (st == 0) STAMP(0);
        STAMP1(st + 1);
        const int so = (st & 3) * P_SLOT;
        bf16x8 v0, v1;
        SB();
        if (st > 0) { cab = MFMA(xf[4 * st - 2], rf[0], cab); PIN(cab); SB(); }
        p1_copy(st, 0);
        xf[4 * st] = ldsfrag(xa[0] + so);
        const bf16x8 r0 = ldsfrag(ra[0] + so);
        if (ROLE == 0) v0 = VFRAG(0);
        SB();
        if (st > 0 && ROLE == 0) { cv = MFMA(xf[4 * st - 2], vf[0], cv); PIN(cv); SB(); }
        xf[4 * st + 1] = ldsfrag(xa[1] + so);
        const bf16x8 r1 = ldsfrag(ra[1] + so);
        if (ROLE == 0) v1 = VFRAG(1);
        SB();
        if (st > 0) { cab = MFMA(xf[4 * st - 1], rf[1], cab); PIN(cab); SB(); }
        p1_copy(st, 1);
        xf[4 * st + 2] = ldsfrag(xa[2] + so);
        rf[0] = ldsfrag(ra[2] + so);
        if (ROLE == 0) vf[0] = VFRAG(2);
        SB();
        if (st > 0 && ROLE == 0) { cv = MFMA(xf[4 * st - 1], vf[1], cv); PIN(cv); SB(); }
        xf[4 * st + 3] = ldsfrag(xa[3] + so);
        rf[1] = ldsfrag(ra[3] + so);
        if (ROLE == 0) vf[1] = VFRAG(3);
        SB();
        cab = MFMA(xf[4 * st], r0, st == 0 ? zero : cab); PIN(cab); SB();
        p1_copy(st, 2);
        if (ROLE == 0) { cv = MFMA(xf[4 * st], v0, st == 0 ? zero : cv); PIN(cv); SB(); }
        cab = MFMA(xf[4 * st + 1], r1, cab); PIN(cab); SB();
        p1_copy(st, 3);
        if (ROLE == 0) { cv = MFMA(xf[4 * st + 1], v1, cv); PIN(cv); SB(); }
    }
#undef VFRAG
    wait_bar<4>();
    STAMP(1);
    STAMP1(9);
#define WFRAG(ks) ((ks) < 24 ? ldsfrag_tr(wa + 16384 + (ks) * 2048, wa + 16384 + (ks) * 2048 + 256) : ldsfrag_tr(wa + ((ks) - 24) * 2048, wa + ((ks) - 24) * 2048 + 256))
    constexpr int PD = 3;
    bf16x8 wf[PD];
#pragma unroll
    for (int ks = 0; ks < PD; ++ks) wf[ks] = WFRAG(ks);
    SB();
    cab = MFMA(xf[30], rf[0], cab); PIN(cab); SB();
    if (ROLE == 0) { cv = MFMA(xf[30], vf[0], cv); PIN(cv); SB(); }
    cab = MFMA(xf[31], rf[1], cab); PIN(cab); SB();
    if (ROLE == 0) { cv = MFMA(xf[31], vf[1], cv); PIN(cv); SB(); }
    const int tile = mi * 4 + wm;
    f32x16 c0;
#pragma unroll
    for (int ks = 0; ks < 32; ++ks) {
        SB();
        if (ks + PD == 8) wait_bar<ROLE ? 7 : 3>();
        if (ks + PD == 16) wait_bar<ROLE ? 4 : 2>();
        SB();
        c0 = MFMA(wf[ks % PD], xf[ks], ks == 0 ? zero : c0); PIN(c0); SB();
        if (ks + PD < 32) { wf[ks % PD] = WFRAG(ks + PD); SB(); }
        const unsigned oa = WS_AB + nj * 262144 + ROLE * 131072 + tile * 2048;
        if (ks == (ROLE ? 4 : 1)) store16c(natural_order(pack8<0>(cab, ROLE ? -1.f : 1.f)), m.sw, m.vl, oa);
        if (ks == (ROLE ? 10 : 7)) store16c(natural_order(pack8<8>(cab, ROLE ? -1.f : 1.f)), m.sw, m.vl, oa + 1024);
        if (ROLE == 0) {
            const unsigned ov = WS_V + 2 * nj * 65536 + tile * 1024, vv = (r >> 4) * 65536 + h * 256 + (r & 15) * 16;
            if (ks == 13) store16c(pack8<0>(cv, 1.f), m.sw, vv, ov);
            if (ks == 16) store16c(pack8<8>(cv, 1.f), m.sw, vv, ov + 512);
        } else {
            if (ks < 4) w_piece(6 + (ks >> 1), (ks & 1) ? w : w - 4);
            if (ks >= 11 && ks < 19) {
                const int st = (ks - 11) >> 2, qr = (ks - 11) & 3;
                dma16p(l0 + M_C + st * 16384 + qr * 4096 + wm * 1024, m.vc - 131072, m.sc, m.oc + ((st + m.rot) & 15) * 128 + qr * 131072);
            }
        }
    }
#undef WFRAG
    constexpr float QS = 0.25f * 1.4426950408889634f;
    const int slot = (r & 19) | ((r & 4) << 1) | ((r & 8) >> 1);
    const unsigned oqk = (ROLE ? WS_K : WS_Q) + 2 * nj * 65536 + tile * 1024, vqk = ROLE ? h * 512 + slot * 16 : m.vl;
    u32x4 qk0 = pack8<0>(c0, ROLE ? 1.f : QS), qk1 = pack8<8>(c0, ROLE ? 1.f : QS);
    PIN(qk0); PIN(qk1);
    STAMP1(11);
    wait_bar<ROLE ? 8 : 0>();
    if (threadIdx.x == 0) post_flag(ws, WS_F + (nj * 16 + mi) * 8, m.epoch);
    STAMP(2);
    STAMP1(10);
    STAMP3(3);
    store16c(qk0, m.sw, vqk, oqk);
    store16c(qk1, m.sw, vqk, oqk + 65536);
}

extern "C" __global__ __launch_bounds__(512, 2) void k_fused(const char* x, const char* cr, const char* ci, const char* wr, const char* wi,
                                                              const char* wqkv, const char* wo, const char* gw, char* ws, char* out) {
    extern __shared__ __attribute__((aligned(16))) char smem[];
    STAMP_BEGIN();
    const lds_ptr lds = (lds_ptr)smem;
    const int lane = threadIdx.x & 63, w = __builtin_amdgcn_readfirstlane(threadIdx.x >> 6);
    {
        u64 pc;
        unsigned len;
        asm volatile("s_getpc_b64 %0\n.Lpc0:\n\ts_mov_b32 %1, .Lpc1-.Lpc0" : "=s"(pc), "=s"(len));
        const u32x4 scode = make_srd((const void*)pc, len);
        asm volatile("s_mov_b32 m0, %0\n\ts_nop 0\n\tbuffer_load_dword %1, %2, 0 offen lds" ::"s"((unsigned)(uintptr_t)lds + P_W0 + w * 256), "v"(threadIdx.x * 96), "s"(scode) : "memory");
    }
    const int g = blockIdx.x & 7, i = blockIdx.x >> 3;
    Ids id;
    id.loc = i;
    id.mi = (g & 3) * 4 + (i & 3); id.nj = (g >> 2) * 8 + (i >> 2);
    id.rot = ROT ? ((i >> 2) + 2 * (i & 3)) & 7 : 0;
    Mid m;
    m.lds = lds; m.l0 = (unsigned)(uintptr_t)lds; m.w = w; m.lane = lane; m.ws = ws;
    ProjSrc ps;
    {
        const int xrow = 8 * w + (lane >> 3);
        ps.vx = xrow * 1024 + (((lane & 7) ^ ((xrow >> 1) & 7)) << 4);
        ps.vr = ps.vx & 0x7fff;
        ps.vq = (lane >> 2) * 3072 + (lane & 3) * 16;
        ps.sx = make_srd(x, S * D * 2); ps.sr = make_srd(w < 4 ? wr : wi, D * D * 2); ps.sq = make_srd(wqkv, D * 3 * D * 2);
    }
    STAMP1(0);
    u32x4 gx, gv;
    u64 gen;
    asm volatile("global_load_dwordx4 %0, %3, off\n\tglobal_load_dwordx4 %1, %4, off\n\tglobal_load_dwordx2 %2, %5, off sc1"
                 : "=&v"(gx), "=&v"(gv), "=&v"(gen) : "v"(x + (size_t)(id.mi * 128 + id.nj * 8 + w) * 1024 + lane * 16), "v"(gw + lane * 16), "v"(ws + WS_GEN) : "memory");
#pragma unroll
    for (int st = 0; st < 2; ++st) {
        proj_stage_dma(m, id, ps, st, 0); proj_stage_dma(m, id, ps, st, 1); proj_stage_dma(m, id, ps, st, 2);
        if (w >= 4) proj_stage_dma(m, id, ps, st, 3);
        if (st == 0) bar();
    }
    SB();
    id.head = g * 4 + (i >> 3); id.qb = i & 7;
    id.sp = g >> 1; id.mt = (g & 1) * 8 + (i >> 2); id.nt = i & 3;
    id.tt = g * 4 + (i >> 3); id.et = i & 7;
    m.sc = make_srd(id.sp < 2 ? cr : ci, S * S * 2); m.sw = make_srd(ws, WS_END); m.so = make_srd(wo, D * D * 2);
    m.rw = __builtin_amdgcn_make_buffer_rsrc(ws, 0, WS_END, 0x00020000);
    m.vc = (ps.vx & 1023) + ((ps.vx & ~1023u) << 2);
    m.vl = lane * 16;
    m.rot = ROT2 ? (ROT2 * id.nt + (i >> 2)) & 15 : 0;
    m.oc = id.mt * 128 * 4096 + (id.sp & 1) * 2048;
    m.oab = WS_AB + (id.nt * 4 + (w >> 1)) * 262144 + (id.sp * 64 + (w & 1) * 2) * 1024;
    {
        const int r = lane & 31, h = lane >> 5;
#pragma unroll
        for (int p = 0; p < 4; ++p) {
            const int sw16 = ((2 * p + h) ^ ((r >> 1) & 7)) << 4;
            ps.xa[p] = lds + (32 * (w & 3) + r) * 128 + sw16;
            ps.ra[p] = lds + 16384 + (w >> 2) * 4096 + r * 128 + sw16;
            PIN(ps.xa[p]); PIN(ps.ra[p]);
        }
        const int wl = (8 * h + ((lane & 15) >> 2)) * 64 + ((lane >> 4) & 1) * 32 + (lane & 3) * 8;
        ps.va = lds + 24576 + wl; ps.wa = lds + P_W1 + (w >> 2) * 1024 + wl;
        PIN(ps.va); PIN(ps.wa); PIN(m.vc); PIN(m.vl);
        if (w < 4) asm volatile("s_waitcnt vmcnt(6)" ::: "memory"); else asm volatile("s_waitcnt vmcnt(8)" ::: "memory");
        asm volatile("" : "+v"(gx), "+v"(gv), "+v"(gen));
        m.epoch = gen + 1;
        float gate = 0.f;
#pragma unroll
        for (int q = 0; q < 4; ++q) gate += bf_lo(gx[q]) * bf_lo(gv[q]) + bf_hi(gx[q]) * bf_hi(gv[q]);
        gate = dpp_sum<0x111, 0xf>(gate); gate = dpp_sum<0x112, 0xf>(gate); gate = dpp_sum<0x114, 0xf>(gate); gate = dpp_sum<0x118, 0xf>(gate);
        gate = dpp_sum<0x142, 0xa>(gate); gate = dpp_sum<0x143, 0xc>(gate);
        gate = __builtin_amdgcn_rcpf(1.f + __expf(-gate));
        if (lane == 63) __hip_atomic_store((float*)(ws + WS_G) + id.mi * 128 + id.nj * 8 + w, gate, __ATOMIC_RELAXED, __HIP_MEMORY_SCOPE_AGENT);
    }
    if (w < 4) proj<0>(m, id, ps);
    else proj<1>(m, id, ps);
    const u64 epoch = m.epoch;
    const int r = lane & 31, h = lane >> 5, qg = w & 3, kh = w >> 2, tb = (w >> 1) & 1, eb = w & 1;
    const int head = id.head, qb = id.qb, tt = id.tt, et = id.et;
    m.okv = ((w & 2) ? WS_V : WS_K) + id.head * 65536 + (kh * 32 + (w & 1)) * 1024;
    m.owo = w * 16384 + id.et * 128;
    m.op = WS_P + id.sp * (2u << 20) + (id.mt * 4 + (w >> 1)) * 32768 + (id.nt * 4 + (w & 1) * 2) * 2048;
    m.oq = WS_Q + id.head * 65536 + (id.qb * 8 + qg * 2 + kh) * 1024;
    m.opp = WS_P + kh * (4u << 20) + (id.tt * 2 + tb) * 32768 + (id.et * 2 + eb) * 2048;
    m.og = WS_G + (id.tt * 64 + tb * 32) * 4;
    m.fb0 = WS_F + 2048 + (id.head >> 1) * 128;
    m.fc0 = WS_F + 6144 + ((id.tt >> 1) * 4 + (id.et >> 1)) * 32;
    m.fc1 = WS_F + ((id.tt & 1) * 128 + (id.tt >> 1)) * 8;
    m.fo2 = WS_F + 2048 + (id.nj * 16 + id.mi) * 8;
    m.fo4 = WS_F + 6144 + ((id.mt * 4 + id.nt) * 4 + id.sp) * 8;
    mid_setup(m);
    {
        const unsigned fa = ((id.nt * 4 + ((lane >> 3) & 3)) * 16 + (id.sp & 1) * 8 + (lane & 7)) * 8, fv = ((id.head >> 1) * 16 + (lane & 15)) * 8;
        const unsigned polla = WS_F + (lane < 32 ? fa : fv);
        if (w == 7) poll_issue(m.pv, m.sw, polla);
        wait_bar<0>();
        if (threadIdx.x == 0) post_flag(ws, m.fo2, epoch);
        STAMP(3);
        STAMP3(4);
        if (w == 7) poll_finish(m.pv, m.sw, polla, epoch);
        bar();
        STAMP3(2);
    }
#pragma unroll
    for (int st = 0; st < 3; ++st) {
        dma16c(m.l0 + M_AB + st * 16384 + w * 2048, m.vl, m.sw, m.oab + ((st + m.rot) & 15) * 4096);
        dma16c(m.l0 + M_AB + st * 16384 + w * 2048 + 1024, m.vl, m.sw, m.oab + ((st + m.rot) & 15) * 4096 + 1024);
        if (st == 0) bar();
    }
    dma16p(m.l0 + M_C + 32768 + w * 1024, m.vc, m.sc, m.oc + ((2 + m.rot) & 15) * 128);
    dma16p(m.l0 + M_C + 32768 + 8192 + w * 1024, m.vc, m.sc, m.oc + ((2 + m.rot) & 15) * 128 + 262144);
    wait_bar<6>();
    STAMP(4);
    STAMP2(0);
    m.fa.a0 = ldsfrag(m.aa); m.fa.c0 = ldsfrag(m.ca[0]); m.fa.a1 = ldsfrag(m.aa + 4096);
    m.fa.a2 = ldsfrag(m.aa + 1024); m.fa.c1 = ldsfrag(m.ca[1]); m.fa.a3 = ldsfrag(m.aa + 4096 + 1024);
    mid_stages<0, LEAD>(m);
    STAMP(5);
    mid_stages<LEAD, 16>(m);
    STAMP(6);
    mid_stages<16, LAST + 1>(m);
    m.o1 = pv_mfma<8>(m.fb.v1, m.s1, m.o1, m.idx);
    STAMP(7);

    {
        const f32x16 zero = {};
        const lds_ptr mine = lds + M_X + w * 3072 + lane * 16, other = lds + M_X + (w ^ 4) * 3072 + lane * 16;
#pragma unroll
        for (int j = 0; j < 3; ++j) *(LAS f32x4*)(mine + j * 1024) = f32x4{m.o1[4 * j], m.o1[4 * j + 1], m.o1[4 * j + 2], m.o1[4 * j + 3]};
        asm volatile("s_waitcnt lgkmcnt(0)\n\ts_barrier" ::: "memory");
        f32x16 o = m.o0;
#pragma unroll
        for (int j = 0; j < 3; ++j) {
            const f32x4 t = *(const LAS f32x4*)(other + j * 1024);
#pragma unroll
            for (int e = 0; e < 4; ++e) o[4 * j + e] += t[e];
        }
        bool ok = o[8] >= 8.271806e-25f && o[8] <= 1.2676506e30f;
#pragma unroll
        for (int e = 0; e < 8; ++e) ok = ok && (__builtin_fabsf(o[e]) <= 3.0e38f);
        if (__builtin_expect(__any(!ok), 0)) {
            const unsigned kb = WS_K + head * 65536, vb = WS_V + head * 65536, vv = h * 256 + (r & 15) * 16;
            float mx = -__builtin_inff();
            for (int t = 0; t < 64; ++t) {
                const u32x4 kf = __builtin_amdgcn_raw_buffer_load_b128(m.rw, m.vl, kb + t * 1024, 16);
                const f32x16 s = MFMA(__builtin_bit_cast(bf16x8, kf), m.q0, zero);
#pragma unroll
                for (int e = 0; e < 16; ++e) mx = __builtin_fmaxf(mx, s[e]);
            }
            mx = __builtin_fmaxf(mx, __shfl_xor(mx, 32));
            const unsigned cw = (r == 16 || r == 20) ? 0x3f803f80u : 0u;
            const u32x4 cv = {cw, cw, cw, cw};
            o = zero;
            for (int t = 0; t < 64; ++t) {
                const u32x4 kf = __builtin_amdgcn_raw_buffer_load_b128(m.rw, m.vl, kb + t * 1024, 16);
                const u32x4 v0 = __builtin_amdgcn_raw_buffer_load_b128(m.rw, vv, vb + t * 1024, 16);
                const u32x4 v1 = __builtin_amdgcn_raw_buffer_load_b128(m.rw, vv, vb + t * 1024 + 512, 16);
                f32x16 s = MFMA(__builtin_bit_cast(bf16x8, kf), m.q0, zero);
#pragma unroll
                for (int e = 0; e < 16; ++e) s[e] = __builtin_amdgcn_exp2f(s[e] - mx);
                o = pv_mfma<0>(__builtin_bit_cast(bf16x8, r < 16 ? v0 : cv), s, o, m.idx);
                o = pv_mfma<8>(__builtin_bit_cast(bf16x8, r < 16 ? v1 : cv), s, o, m.idx);
            }
        }
        store16c(pack8<0>(o, __builtin_amdgcn_rcpf(o[8])), m.sw, m.vl, WS_O + (qb * 8 + qg * 2 + kh) * 32768 + head * 1024);
    }
    if (w == 6) poll_block(m.sw, (lane & 8) ? m.fc0 + (lane & 3) * 8 : m.fc1 + (lane & 7) * 128, epoch);
    asm volatile("s_waitcnt vmcnt(0) lgkmcnt(0)\n\ts_barrier" ::: "memory");
    STAMP(8);
    STAMP3(7);
    if (threadIdx.x == 0) post_flag(ws, WS_F + 4096 + (qb * 32 + head) * 8, epoch);
    asm volatile("buffer_load_dwordx4 %0, %5, %6, %7 offen sc1\n\tbuffer_load_dwordx4 %1, %5, %6, %7 offen offset:1024 sc1\n\t"
                 "buffer_load_dwordx4 %2, %5, %6, %8 offen sc1\n\tbuffer_load_dwordx4 %3, %5, %6, %8 offen offset:1024 sc1\n\t"
                 "buffer_load_dword %4, %9, %6, %10 offen sc1"
                 : "=&v"(m.p0), "=&v"(m.p1), "=&v"(m.p2), "=&v"(m.p3), "=&v"(m.gate)
                 : "v"(m.vl), "s"(m.sw), "s"(m.opp), "s"(m.opp + (2u << 20)), "v"(r * 4), "s"(m.og) : "memory");
    if (w == 7) poll_block(m.sw, WS_F + 4096 + ((tt >> 2) * 32 + r) * 8, epoch);
    bar();
    STAMP(9);
    STAMP3(8);

#pragma unroll
    for (int c = 0; c < 4; ++c) {
        const int ks = c * 4 + (w & 3);
        dma16c(m.l0 + (kh ? o_chunk(1) : o_chunk(0)) + ks * 1024, m.vl, m.sw, WS_O + (tt * 2) * 32768 + (kh * 16 + ks) * 1024);
        dma16c(m.l0 + (kh ? o_chunk(3) : o_chunk(2)) + ks * 1024, m.vl, m.sw, WS_O + (tt * 2 + 1) * 32768 + (kh * 16 + ks) * 1024);
    }
    {
        const int q = (lane & 15) >> 2;
        const lds_ptr oa = lds + (tb ? (kh ? o_chunk(3) : o_chunk(2)) : (kh ? o_chunk(1) : o_chunk(0))) + lane * 16;
        const int wl = (4 * h + q) * 128 + ((eb * 64) ^ ((q & 2) << 5)) + ((lane >> 4) & 1) * 32 + (lane & 3) * 8;
        const lds_ptr wa0 = lds + (kh ? wo_chunk(2) : wo_chunk(0)) + wl, wa1 = lds + (kh ? wo_chunk(3) : wo_chunk(1)) + wl;
        bf16x8 wf[16];
#pragma unroll
        for (int ks = 0; ks < 16; ++ks) {
            const lds_ptr p = (ks < 8 ? wa0 : wa1) + (ks & 7) * 2048;
            wf[ks] = ldsfrag_tr(p, p + 1024);
        }
        asm volatile("s_waitcnt vmcnt(8)" : "+v"(m.p0), "+v"(m.p1), "+v"(m.p2), "+v"(m.p3), "+v"(m.gate)::"memory");
        f32x16 yv;
#pragma unroll
        for (int j = 0; j < 8; ++j) {
            const unsigned a = j < 4 ? m.p0[j & 3] : m.p1[j & 3], b = j < 4 ? m.p2[j & 3] : m.p3[j & 3];
            yv[2 * j] = m.gate * (bf_lo(a) + bf_lo(b));
            yv[2 * j + 1] = m.gate * (bf_hi(a) + bf_hi(b));
        }
        PIN(yv);
        f32x16 acc = {};
#pragma unroll
        for (int c = 0; c < 4; ++c) {
            SB();
            if (c == 0) wait_bar<6>(); else if (c == 1) wait_bar<4>(); else if (c == 2) wait_bar<2>(); else wait_bar<0>();
            bf16x8 of[4];
#pragma unroll
            for (int p = 0; p < 4; ++p) of[p] = ldsfrag(oa + (c * 4 + p) * 1024);
            SB();
#pragma unroll
            for (int p = 0; p < 4; ++p) acc = MFMA(wf[c * 4 + p], of[p], acc);
        }
        STAMP(10);
        const float og = 1.f - m.gate;
        const lds_ptr st = lds + M_X - 2048 + kh * (64 * O_PITCH) + (tb * 32 + r) * O_PITCH + (eb * 32 + 4 * h) * 4;
        asm volatile("s_waitcnt lgkmcnt(0)\n\ts_barrier" ::: "memory");
#pragma unroll
        for (int j = 0; j < 4; ++j) {
            const f32x4 v = {yv[4 * j] + og * acc[4 * j], yv[4 * j + 1] + og * acc[4 * j + 1], yv[4 * j + 2] + og * acc[4 * j + 2], yv[4 * j + 3] + og * acc[4 * j + 3]};
            *(LAS f32x4*)(st + j * 32) = v;
        }
        asm volatile("s_waitcnt lgkmcnt(0)\n\ts_barrier" ::: "memory");
        const int row = threadIdx.x >> 3, ch = threadIdx.x & 7;
        const lds_ptr rd = lds + M_X - 2048 + row * O_PITCH + ch * 32;
        const f32x4 a0 = *(const LAS f32x4*)rd, a1 = *(const LAS f32x4*)(rd + 16);
        const f32x4 b0 = *(const LAS f32x4*)(rd + 64 * O_PITCH), b1 = *(const LAS f32x4*)(rd + 64 * O_PITCH + 16);
        const u32x4 o = {pk2(a0[0] + b0[0], a0[1] + b0[1]), pk2(a0[2] + b0[2], a0[3] + b0[3]), pk2(a1[0] + b1[0], a1[1] + b1[1]), pk2(a1[2] + b1[2], a1[3] + b1[3])};
        asm volatile("global_store_dwordx4 %0, %1, off sc1\n\ts_nop 1" ::"v"(out + (size_t)(tt * 64 + row) * 1024 + et * 128 + ch * 16), "v"(o) : "memory");
    }
    asm volatile(".Lpc1:");
    if (blockIdx.x == 0 && w < 4) poll_block(m.sw, WS_F + 2048 + threadIdx.x * 8, epoch);
    if (blockIdx.x == 0) {
        bar();
        if (threadIdx.x == 0) post_flag(ws, WS_GEN, epoch);
    }
    STAMP_END();
}

extern "C" void kernel_launch(void* const* d_in, const int* in_sizes, int n_in, void* d_out, int out_size, void* d_ws, size_t ws_size, hipStream_t stream) {
    (void)in_sizes; (void)n_in; (void)out_size; (void)ws_size;
    (void)hipFuncSetAttribute((const void*)k_fused, hipFuncAttributeMaxDynamicSharedMemorySize, LDS_BYTES);
    k_fused<<<256, 512, LDS_BYTES, stream>>>((const char*)d_in[0], (const char*)d_in[1], (const char*)d_in[2], (const char*)d_in[3], (const char*)d_in[4],
                                             (const char*)d_in[6], (const char*)d_in[7], (const char*)d_in[8], (char*)d_ws + WSOFF, (char*)d_out);
}
```

```cpp
#include <hip/hip_runtime.h>
#include <hip/hip_bf16.h>
#include <stdint.h>

typedef unsigned long long u64;
typedef float f32x16 __attribute__((ext_vector_type(16)));
typedef float f32x8 __attribute__((ext_vector_type(8)));
typedef float f32x4 __attribute__((ext_vector_type(4)));
typedef __bf16 bf16x8 __attribute__((ext_vector_type(8)));
typedef __bf16 bf16x16 __attribute__((ext_vector_type(16)));
typedef unsigned u32x4 __attribute__((ext_vector_type(4)));
typedef short s16x4 __attribute__((ext_vector_type(4)));
#define LAS __attribute__((address_space(3)))
typedef LAS char* lds_ptr;
#define DI __device__ __forceinline__
#define SB() __builtin_amdgcn_sched_barrier(0)
#define PIN(x) asm volatile("" : "+v"(x))
#define MFMA(a, b, c) __builtin_amdgcn_mfma_f32_32x32x16_bf16((a), (b), (c), 0, 0, 0)

constexpr int S = 2048, D = 512;
constexpr int LEAD = 2;
constexpr int SH = 6;
constexpr unsigned WS_Q = 0;
constexpr unsigned WS_K = 2u << 20;
constexpr unsigned WS_V = 4u << 20;
constexpr unsigned WS_AB = 6u << 20;
constexpr unsigned WS_O = 10u << 20;
constexpr unsigned WS_P = 12u << 20;
constexpr unsigned WS_G = 20u << 20;
constexpr unsigned WS_F = WS_G + 8192;
constexpr unsigned WS_GEN = WS_F + 8192;
constexpr unsigned WS_END = WS_GEN + 256;
constexpr unsigned SPIN_CAP = 1u << 20;

DI u32x4 make_srd(const void* p, unsigned bytes) {
    const u64 a = (u64)p;
    u32x4 r = {(unsigned)a, (unsigned)(a >> 32) & 0xffffu, bytes, 0x00020000u};
    return r;
}
DI void dma16(unsigned lds, unsigned voff, u32x4 srd, unsigned soff) {
    asm volatile("s_mov_b32 m0, %0\n\ts_nop 0\n\tbuffer_load_dwordx4 %1, %2, %3 offen lds" ::"s"(lds), "v"(voff), "s"(srd), "s"(soff) : "memory");
}
DI void dma16c(unsigned lds, unsigned voff, u32x4 srd, unsigned soff) {
    asm volatile("s_mov_b32 m0, %0\n\ts_nop 0\n\tbuffer_load_dwordx4 %1, %2, %3 offen sc1 lds" ::"s"(lds), "v"(voff), "s"(srd), "s"(soff) : "memory");
}
DI void store16c(u32x4 v, u32x4 srd, unsigned voff, unsigned soff) {
    asm volatile("buffer_store_dwordx4 %0, %1, %2, %3 offen sc1\n\ts_nop 1" ::"v"(v), "v"(voff), "s"(srd), "s"(soff) : "memory");
}
DI void post_flag(char* ws, unsigned off, u64 epoch) { __hip_atomic_store((u64*)(ws + off), epoch, __ATOMIC_RELAXED, __HIP_MEMORY_SCOPE_AGENT); }
DI void poll_issue(u64& v, u32x4 srd, unsigned voff) { asm volatile("buffer_load_dwordx2 %0, %1, %2, 0 offen sc1" : "=v"(v) : "v"(voff), "s"(srd) : "memory"); }
DI void poll_finish(u64 v, u32x4 srd, unsigned voff, u64 epoch) {
    asm volatile("" : "+v"(v));
    unsigned spins = 0;
    while (!__all(v == epoch)) {
        if (++spins > SPIN_CAP) break;
        __builtin_amdgcn_s_sleep(1);
        asm volatile("buffer_load_dwordx2 %0, %1, %2, 0 offen sc1\n\ts_waitcnt vmcnt(0)" : "=&v"(v) : "v"(voff), "s"(srd) : "memory");
    }
}
DI void poll_block(u32x4 srd, unsigned voff, u64 epoch) {
    u64 v;
    asm volatile("buffer_load_dwordx2 %0, %1, %2, 0 offen sc1\n\ts_waitcnt vmcnt(0)" : "=&v"(v) : "v"(voff), "s"(srd) : "memory");
    poll_finish(v, srd, voff, epoch);
}
DI bf16x8 ldsfrag(lds_ptr p) { return *(const LAS bf16x8*)p; }
DI bf16x8 ldsfrag_tr(lds_ptr p0, lds_ptr p1) {
    const s16x4 lo = __builtin_amdgcn_ds_read_tr16_b64_v4i16((LAS s16x4*)p0);
    const s16x4 hi = __builtin_amdgcn_ds_read_tr16_b64_v4i16((LAS s16x4*)p1);
    return __builtin_bit_cast(bf16x8, __builtin_shufflevector(lo, hi, 0, 1, 2, 3, 4, 5, 6, 7));
}
DI unsigned pk2(float lo, float hi) {
    typedef __bf16 bf16x2 __attribute__((ext_vector_type(2)));
    typedef float f32x2 __attribute__((ext_vector_type(2)));
    const f32x2 v = {lo, hi};
    return __builtin_bit_cast(unsigned, __builtin_convertvector(v, bf16x2));
}
template <int B> DI u32x4 pack8(const f32x16& v, float sc) {
    u32x4 r = {pk2(v[B] * sc, v[B + 1] * sc), pk2(v[B + 2] * sc, v[B + 3] * sc), pk2(v[B + 4] * sc, v[B + 5] * sc), pk2(v[B + 6] * sc, v[B + 7] * sc)};
    return r;
}
DI u32x4 natural_order(u32x4 p) {
    const auto s0 = __builtin_amdgcn_permlane32_swap(p[0], p[2], false, false);
    const auto s1 = __builtin_amdgcn_permlane32_swap(p[1], p[3], false, false);
    u32x4 r = {s0[0], s1[0], s0[1], s1[1]};
    return r;
}
DI float bf_lo(unsigned u) { return __uint_as_float(u << 16); }
DI float bf_hi(unsigned u) { return __uint_as_float(u & 0xffff0000u); }
template <int B> DI f32x16 pv_mfma(bf16x8 vfrag, const f32x16& p, f32x16 o, int idx) {
    const f32x8 h = __builtin_shufflevector(p, p, B, B + 1, B + 2, B + 3, B + 4, B + 5, B + 6, B + 7);
    return __builtin_amdgcn_smfmac_f32_32x32x32_bf16(vfrag, __builtin_bit_cast(bf16x16, h), o, idx, 0, 0);
}
template <int N> DI void wait_vm() { asm volatile("s_waitcnt vmcnt(%0) lgkmcnt(0)" ::"i"(N) : "memory"); }
DI void bar() { asm volatile("s_barrier" ::: "memory"); }
template <int N> DI void wait_bar() { wait_vm<N>(); bar(); }
template <int N> DI void wait_bar_vm() { asm volatile("s_waitcnt vmcnt(%0)\n\ts_barrier" ::"i"(N) : "memory"); }

constexpr int P_SLOT = 28672, P_W0 = 114688, P_W1 = 98304;
constexpr int M_C = 0, M_AB = 49152, M_KV = 98304, M_CONST = M_KV + 3 * 8192, M_X = 131072;
constexpr int LDS_BYTES = 163840, O_PITCH = 272;
constexpr int wo_chunk(int c) { return c == 0 ? 16384 : c == 1 ? 65536 : c == 2 ? 32768 : 81920; }
constexpr int o_chunk(int c) { return c == 0 ? 0 : c == 1 ? 49152 : c == 2 ? 98304 : 114688; }
constexpr int LAST = LEAD + 15;
constexpr int ops_after(int j) { return (j <= 14 ? 4 : 0) + (j == 0 ? 3 : 0) + (j >= LEAD && j + 3 - LEAD < 16 ? 1 : 0) + (j == 15 ? 4 : 0); }

struct Frag { bf16x8 k, v0, v1, a0, a1, c0, a2, a3, c1; };

struct Mid {
    lds_ptr lds, ka, va[3], aa, ca[4];
    unsigned l0, vc, vl, vwo, oc, oab, okv, owo, w;
    u32x4 sc, sw, so;
    __amdgpu_buffer_rsrc_t rw;
    char* ws;
    unsigned op, fo2, fo4, oq, opp, og, fb0, fc0, fc1, pollv;
    u64 epoch, pv;
    int lane, idx;
    bf16x8 q0, q1;
    f32x16 o0, o1, y0, y1, s0, s1;
    Frag fa, fb;
    u32x4 p0, p1, p2, p3;
    float gate;
};

template <int J, int P> DI void mid_dma(Mid& m) {
    constexpr int SL = J % 3;
    if constexpr (J < 0 || P > 4) {
    } else if constexpr (J + 3 < 16) {
        constexpr int ks = J + 3;
        if constexpr (P == 1) dma16(m.l0 + M_C + SL * 16384 + m.w * 1024, m.vc, m.sc, m.oc + ks * 128);
        if constexpr (P == 2) dma16(m.l0 + M_C + SL * 16384 + 8192 + m.w * 1024, m.vc, m.sc, m.oc + ks * 128 + 262144);
        if constexpr (P == 3) dma16c(m.l0 + M_AB + SL * 16384 + m.w * 2048, m.vl, m.sw, m.oab + ks * 4096);
        if constexpr (P == 4) dma16c(m.l0 + M_AB + SL * 16384 + m.w * 2048 + 1024, m.vl, m.sw, m.oab + ks * 4096 + 1024);
    } else if constexpr (J <= 14 && P >= 1) {
        constexpr int c = (J - 13) * 2 + (P - 1) / 2, hf = (P - 1) & 1;
        dma16(m.l0 + wo_chunk(c) + m.w * 2048 + hf * 1024, m.vwo, m.so, m.owo + c * 131072 + hf * 8192);
    }
    if constexpr (P == 0) {
        if constexpr (J == 0) dma16c(m.l0 + M_KV + m.w * 1024, m.vl, m.sw, m.okv);
        if constexpr (J >= LEAD && J + 3 - LEAD < 16) dma16c(m.l0 + M_KV + ((J - LEAD) % 3) * 8192 + m.w * 1024, m.vl, m.sw, m.okv + (J + 3 - LEAD) * 2048);
    }
    if constexpr (P == 5 && J == 0) {
        dma16c(m.l0 + M_KV + 8192 + m.w * 1024, m.vl, m.sw, m.okv + 2048);
        dma16c(m.l0 + M_KV + 16384 + m.w * 1024, m.vl, m.sw, m.okv + 4096);
    }
}

DI void mid_setup(Mid& m) {
    const int ln = m.lane, rr = ln & 31, hh = ln >> 5, kh = m.w >> 2;
    const int cslot = (rr == 16) ? 0 : (rr == 20) ? 4 : (rr < 20 || rr >= 28) ? 1 : 5;
#pragma unroll
    for (int s = 0; s < 3; ++s) { m.va[s] = (rr < 16) ? m.lds + M_KV + s * 8192 + kh * 4096 + 2048 + hh * 256 + rr * 16 : m.lds + M_CONST + cslot * 16; PIN(m.va[s]); }
#pragma unroll
    for (int p = 0; p < 4; ++p) { m.ca[p] = m.lds + M_C + ((m.w >> 1) * 32 + rr) * 128 + (((2 * p + hh) ^ ((rr >> 1) & 7)) << 4); PIN(m.ca[p]); }
    m.aa = m.lds + M_AB + (m.w & 1) * 8192 + ln * 16;
    m.ka = m.lds + M_KV + kh * 4096 + ln * 16;
    m.vwo = (ln >> 3) * 1024 + (((ln & 7) ^ (((ln >> 3) & 2) << 1)) << 4);
    m.idx = (int)0xDDDDDDDD;
    m.pollv = WS_F + 2048 + (((m.fb0 - (1 + ((ln >> 4) & 1)) * SH) & 15) * 16 + (ln & 15)) * 8;
    m.o0 = f32x16{}; m.o1 = f32x16{};
    PIN(m.aa); PIN(m.ka); PIN(m.vwo); PIN(m.idx); PIN(m.pollv); PIN(m.o0); PIN(m.o1);
    if (m.w >= 6) {
        const unsigned one2 = ((ln & 11) == 0) ? 0x3f803f80u : 0u;
        const u32x4 cv = {one2, one2, one2, one2};
        *(LAS u32x4*)(m.lds + M_CONST + (m.w - 6) * 1024 + ln * 16) = cv;
    }
}

constexpr char PAT[] = "DeeekvPeeeQeeewmDeee21Veeee|yx0DeeemPeee3QeeeDeeemVeeee";
constexpr int pat_count(int upto, char c) { int n = 0; for (int i = 0; i < upto; ++i) n += PAT[i] == c; return n; }

template <int J, int U, int I = 0> DI void half_stage(Mid& m, Frag& F, Frag& N) {
    constexpr int NJ = U ? J + 1 : J, NU = U ^ 1;
    constexpr bool DFT = J < 16, ATT = J >= LEAD, NDFT = NJ < 16, NATT = NJ >= LEAD && NJ <= LAST, FIRST = J == LEAD && U == 0;
    constexpr int CS = (NJ % 3) * 16384, KS = NATT ? (NJ - LEAD) % 3 : 0;
    constexpr char t = PAT[I];
    if constexpr (t != 0) {
        constexpr int T = pat_count(I, '|'), n = pat_count(I, t);
        const f32x16 zero = {};
        f32x16 &sa = T ? m.s1 : m.s0, &sb = T ? m.s0 : m.s1, &oa = T ? m.o1 : m.o0, &ob = T ? m.o0 : m.o1;
        if constexpr (t == 'e' && ATT) { sa[n & 15] = __builtin_amdgcn_exp2f(sa[n & 15]); if constexpr (PAT[I + 1] != 'e') PIN(sa); }
        if constexpr (t == 'P' && ATT && !(FIRST && T == 0)) { ob = pv_mfma<8>(T ? F.v1 : N.v1, sb, ob, m.idx); PIN(ob); }
        if constexpr (t == 'V' && ATT) { oa = pv_mfma<0>(F.v0, sa, oa, m.idx); PIN(oa); }
        if constexpr (t == 'Q' && (T ? NATT : ATT)) { sb = MFMA(T ? N.k : F.k, T ? m.q0 : m.q1, zero); PIN(sb); }
        if constexpr (t == 'D' && DFT) {
            f32x16& y = (n & 1) ? m.y1 : m.y0;
            y = MFMA(n == 0 ? F.a0 : n == 1 ? F.a1 : n == 2 ? F.a2 : F.a3, n < 2 ? F.c0 : F.c1, (J == 0 && U == 0 && n < 2) ? zero : y);
            PIN(y);
        }
        if constexpr (NATT) {
            if constexpr (t == 'k') N.k = ldsfrag(m.ka + KS * 8192 + NU * 1024);
            if constexpr (t == 'v') N.v0 = ldsfrag(m.va[KS] + NU * 1024);
            if constexpr (t == 'w') N.v1 = ldsfrag(m.va[KS] + NU * 1024 + 512);
        }
        if constexpr (NDFT) {
            if constexpr (t == '0') N.a0 = ldsfrag(m.aa + CS + NU * 2048);
            if constexpr (t == '1') N.a1 = ldsfrag(m.aa + CS + NU * 2048 + 4096);
            if constexpr (t == '2') N.a2 = ldsfrag(m.aa + CS + NU * 2048 + 1024);
            if constexpr (t == '3') N.a3 = ldsfrag(m.aa + CS + NU * 2048 + 4096 + 1024);
            if constexpr (t == 'x') N.c0 = ldsfrag(m.ca[2 * NU] + CS);
            if constexpr (t == 'y') N.c1 = ldsfrag(m.ca[2 * NU + 1] + CS);
        }
        if constexpr (t == 'm') { if constexpr (U) mid_dma<J, n>(m); else mid_dma<J - 1, 3 + n>(m); }
        if constexpr (t != 'e' || PAT[I + 1] != 'e') SB();
        half_stage<J, U, I + 1>(m, F, N);
    }
}

template <int J> DI void mid_stage(Mid& m) {
    half_stage<J, 0>(m, m.fa, m.fb);
    if constexpr (J < LAST) {
        if constexpr (J == 0) { wait_vm<4>(); if (m.w == 7) { wait_vm<0>(); poll_finish(m.pv, m.sw, m.pollv, m.epoch); } }
        else if constexpr (J == 1) wait_vm<6>();
        else wait_vm<ops_after(J - 1)>();
        bar();
        if constexpr (J == 1) asm volatile("" : "+v"(m.q0), "+v"(m.q1));
    }
    half_stage<J, 1>(m, m.fb, m.fa);
    if constexpr (J == 15) {
        store16c(pack8<0>(m.y0, 1.f), m.sw, m.vl, m.op);
        store16c(pack8<8>(m.y0, 1.f), m.sw, m.vl, m.op + 1024);
        store16c(pack8<0>(m.y1, 1.f), m.sw, m.vl, m.op + 2048);
        store16c(pack8<8>(m.y1, 1.f), m.sw, m.vl, m.op + 3072);
    }
}
template <int J, int E> DI void mid_stages(Mid& m) {
    if constexpr (J < E) { mid_stage<J>(m); mid_stages<J + 1, E>(m); }
}

struct Ids { int mi, nj, head, qb, sp, mt, nt, tt, et; unsigned rot; };

struct ProjSrc { u32x4 sx, sr, si, sq; unsigned vx, vq; lds_ptr xa[4], ra[4], va, wa; };
constexpr int NA = 2, NWA = 1;
constexpr int ncopy(int role) { return role ? 7 - NA : NA; }
template <int ROLE> DI void proj_stage_dma(const Mid& m, const Ids& id, const ProjSrc& ps, int st, int q) {
    const unsigned v = m.w & 3, d = m.l0 + (st & 3) * P_SLOT + v * 1024, kr = (st + id.rot) & 7;
    const int xq = ROLE ? NA + q : q;
    if (q >= ncopy(ROLE)) return;
    if (xq < 4) dma16(d + xq * 4096, ps.vx, ps.sx, id.mi * 131072 + kr * 128 + xq * 32768);
    else if (xq == 4) dma16(d + 16384, ps.vx, ps.sr, id.nj * 32768 + kr * 128);
    else if (xq == 5) dma16(d + 20480, ps.vx, ps.si, id.nj * 32768 + kr * 128);
    else dma16(d + 24576, ps.vq, ps.sq, (kr * 4 + v) * 49152 + id.nj * 64);
}
template <int CTRL, int RMASK> DI float dpp_sum(float v) {
    return v + __int_as_float(__builtin_amdgcn_update_dpp(0, __float_as_int(v), CTRL, RMASK, 0xf, true));
}

struct Pass1 { bf16x8 xf[32], rf[4], vf[4]; f32x16 cab, cv; };
constexpr char P1PAT[2][32] = {"ambxrvabxrvmabxrvabxrv", "amxrmamxramxramxr"};
constexpr int p1_count(int role, int upto, char c) { int n = 0; for (int i = 0; i < upto; ++i) n += P1PAT[role][i] == c; return n; }
template <int ROLE, int ST, int I, class CP> DI void p1_stage(Pass1& s, const ProjSrc& ps, CP& copy) {
    constexpr char t = P1PAT[ROLE][I];
    if constexpr (t != 0) {
        constexpr int n = p1_count(ROLE, I, t), so = (ST & 3) * P_SLOT;
        const f32x16 zero = {};
        if constexpr (t == 'a' && ST > 0) { s.cab = MFMA(s.xf[4 * ST - 4 + n], s.rf[n], (ST == 1 && n == 0) ? zero : s.cab); PIN(s.cab); }
        if constexpr (t == 'b' && ST > 0) { s.cv = MFMA(s.vf[n], s.xf[4 * ST - 4 + n], (ST == 1 && n == 0) ? zero : s.cv); PIN(s.cv); }
        if constexpr (ST < 8) {
            if constexpr (t == 'x') s.xf[4 * ST + n] = ldsfrag(ps.xa[n] + so);
            if constexpr (t == 'r') s.rf[n] = ldsfrag(ps.ra[n] + so);
            if constexpr (t == 'v') s.vf[n] = ldsfrag_tr(ps.va + so + n * 1024, ps.va + so + n * 1024 + 256);
            if constexpr (t == 'm') copy(ST, n);
        }
        SB();
        p1_stage<ROLE, ST, I + 1>(s, ps, copy);
    }
}
template <int ROLE, int ST, int NS, int NW, class CP> DI void p1_stages(Pass1& s, const ProjSrc& ps, CP& copy) {
    if constexpr (ST < 8) {
        wait_bar<ST <= 1 ? 2 * NS + 1 : ST <= 5 ? 2 * NS : ST == 6 ? NS + NW : 2 * NW>();
        SB();
        p1_stage<ROLE, ST, 0>(s, ps, copy);
        p1_stages<ROLE, ST + 1, NS, NW>(s, ps, copy);
    }
}

template <int ROLE>
DI void proj(Mid& m, const Ids& id, const ProjSrc& ps) {
    const int lane = m.lane, w = m.w, r = lane & 31, h = lane >> 5, wm = w & 3, mi = id.mi, nj = id.nj;
    const unsigned l0 = m.l0;
    char* ws = m.ws;
    constexpr int NS = ncopy(ROLE), NW = ROLE ? 4 - NWA : NWA;
    auto w_piece = [&](int i, unsigned wv) {
        dma16(l0 + (i < 6 ? P_W0 : P_W1 - 49152) + (8 * i + wv) * 1024, ps.vq, ps.sq, ((4 * i + (wv >> 1) + 4 * id.rot) & 31) * 49152 + 2048 - (wv & 1) * 1024 + ((nj + (1 + (wv & 1)) * SH) & 15) * 64);
    };
    const lds_ptr wa = ps.wa;
    auto p1_copy = [&](int st, int q) {
        if (st <= 4) proj_stage_dma<ROLE>(m, id, ps, st + 3, q);
        else if (q < NW) { const int e = ROLE ? NWA + q : q; w_piece(2 * (st - 5) + (e >> 1), (e & 1) * 4 + wm); }
        SB();
    };
    const f32x16 zero = {};
    Pass1 s1;
    bf16x8* xf = s1.xf;
    f32x16 &cab = s1.cab, &cv = s1.cv;
    SB();
#pragma unroll
    for (int q = 0; q < NS; ++q) proj_stage_dma<ROLE>(m, id, ps, 2, q);
    p1_stages<ROLE, 0, NS, NW>(s1, ps, p1_copy);
    wait_bar<2 * NW>();
#define WFRAG(ks) ((ks) < 24 ? ldsfrag_tr(wa + 16384 + (ks) * 2048, wa + 16384 + (ks) * 2048 + 256) : ldsfrag_tr(wa + ((ks) - 24) * 2048, wa + ((ks) - 24) * 2048 + 256))
    constexpr int PD = 3;
    bf16x8 wf[PD];
#pragma unroll
    for (int ks = 0; ks < PD; ++ks) wf[ks] = WFRAG(ks);
    SB();
    p1_stage<ROLE, 8, 0>(s1, ps, p1_copy);
    const int tile = mi * 4 + wm;
    f32x16 c0;
#pragma unroll
    for (int ks = 0; ks < 32; ++ks) {
        SB();
        if (ks + PD == 8) wait_bar_vm<NW + (ROLE ? 5 : 1)>();
        if (ks + PD == 16) wait_bar_vm<ROLE ? 4 : 2>();
        SB();
        c0 = ROLE ? MFMA(wf[ks % PD], xf[ks], ks == 0 ? zero : c0) : MFMA(xf[ks], wf[ks % PD], ks == 0 ? zero : c0); PIN(c0); SB();
        if (ks + PD < 32) { wf[ks % PD] = WFRAG(ks + PD); SB(); }
        const unsigned oa = WS_AB + nj * 262144 + ROLE * 131072 + tile * 2048;
        if (ks == (ROLE ? 4 : 1)) store16c(natural_order(pack8<0>(cab, ROLE ? -1.f : 1.f)), m.sw, m.vl, oa);
        if (ks == (ROLE ? 10 : 7)) store16c(natural_order(pack8<8>(cab, ROLE ? -1.f : 1.f)), m.sw, m.vl, oa + 1024);
        if (ROLE == 0) {
            constexpr float QS = 0.25f * 1.4426950408889634f;
            const unsigned oq = WS_Q + 2 * nj * 65536 + tile * 1024;
            if (ks == 13) store16c(pack8<0>(cv, QS), m.sw, m.vl, oq);
            if (ks == 16) store16c(pack8<8>(cv, QS), m.sw, m.vl, oq + 65536);
        } else {
            if (ks < 4) w_piece(6 + (ks >> 1), (ks & 1) ? w : w - 4);
            if (ks >= 11 && ks < 23) {
                const int st = (ks - 11) >> 2, qr = (ks - 11) & 3;
                dma16(l0 + M_C + st * 16384 + qr * 4096 + wm * 1024, m.vc - 131072, m.sc, m.oc + st * 128 + qr * 131072);
            }
        }
    }
#undef WFRAG
    const int slot = (r & 19) | ((r & 4) << 1) | ((r & 8) >> 1);
    const unsigned oqk = (ROLE ? WS_K : WS_V) + 2 * ((nj + (1 + ROLE) * SH) & 15) * 65536 + tile * 1024;
    const unsigned vqk = ROLE ? h * 512 + slot * 16 : (r >> 4) * 65536 + h * 256 + (r & 15) * 16;
    u32x4 qk0 = pack8<0>(c0, 1.f), qk1 = pack8<8>(c0, 1.f);
    PIN(qk0); PIN(qk1);
    wait_bar<ROLE ? 12 : 0>();
    if (threadIdx.x == 0) post_flag(ws, WS_F + (nj * 16 + mi) * 8, m.epoch);
    store16c(qk0, m.sw, vqk, oqk);
    store16c(qk1, m.sw, vqk, oqk + (ROLE ? 65536 : 512));
}

extern "C" __global__ __launch_bounds__(512, 2) void k_fused(const char* x, const char* cr, const char* ci, const char* wr, const char* wi,
                                                              const char* wqkv, const char* wo, const char* gw, char* ws, char* out) {
    extern __shared__ __attribute__((aligned(16))) char smem[];
    const lds_ptr lds = (lds_ptr)smem;
    const int lane = threadIdx.x & 63, w = __builtin_amdgcn_readfirstlane(threadIdx.x >> 6);
    {
        u64 pc;
        unsigned len;
        asm volatile("s_getpc_b64 %0\n.Lpc0:\n\ts_mov_b32 %1, .Lpc1-.Lpc0" : "=s"(pc), "=s"(len));
        const u32x4 scode = make_srd((const void*)pc, len);
        asm volatile("s_mov_b32 m0, %0\n\ts_nop 0\n\tbuffer_load_dword %1, %2, 0 offen lds" ::"s"((unsigned)(uintptr_t)lds + P_W0 + w * 256), "v"(threadIdx.x * 96), "s"(scode) : "memory");
    }
    const int g = blockIdx.x & 7, i = blockIdx.x >> 3;
    Ids id;
    id.mi = (g & 3) * 4 + (i & 3); id.nj = (g >> 2) * 8 + (i >> 2);
    id.rot = ((i >> 2) + 2 * (i & 3)) & 7;
    Mid m;
    m.lds = lds; m.l0 = (unsigned)(uintptr_t)lds; m.w = w; m.lane = lane; m.ws = ws;
    ProjSrc ps;
    {
        const int xrow = 8 * (w & 3) + (lane >> 3);
        ps.vx = xrow * 1024 + (((lane & 7) ^ ((xrow >> 1) & 7)) << 4);
        ps.vq = (lane >> 2) * 3072 + (lane & 3) * 16;
        ps.sx = make_srd(x, S * D * 2); ps.sr = make_srd(wr, D * D * 2); ps.si = make_srd(wi, D * D * 2); ps.sq = make_srd(wqkv, D * 3 * D * 2);
    }
    u32x4 gx, gv;
    u64 gen;
    asm volatile("global_load_dwordx4 %0, %3, off\n\tglobal_load_dwordx4 %1, %4, off\n\tglobal_load_dwordx2 %2, %5, off sc1"
                 : "=&v"(gx), "=&v"(gv), "=&v"(gen) : "v"(x + (size_t)(id.mi * 128 + id.nj * 8 + w) * 1024 + lane * 16), "v"(gw + lane * 16), "v"(ws + WS_GEN) : "memory");
#pragma unroll
    for (int st = 0; st < 2; ++st) {
        if (w < 4) { _Pragma("unroll") for (int q = 0; q < ncopy(0); ++q) proj_stage_dma<0>(m, id, ps, st, q); }
        else { _Pragma("unroll") for (int q = 0; q < ncopy(1); ++q) proj_stage_dma<1>(m, id, ps, st, q); }
        if (st == 0) bar();
    }
    SB();
    id.head = g * 4 + (i >> 3); id.qb = i & 7;
    id.sp = g >> 1; id.mt = (g & 1) * 8 + (i >> 2); id.nt = i & 3;
    id.tt = g * 4 + (i >> 3); id.et = i & 7;
    m.sc = make_srd(id.sp < 2 ? cr : ci, S * S * 2); m.sw = make_srd(ws, WS_END); m.so = make_srd(wo, D * D * 2);
    m.rw = __builtin_amdgcn_make_buffer_rsrc(ws, 0, WS_END, 0x00020000);
    m.vc = (ps.vx & 1023) + ((ps.vx & ~1023u) << 2) + (w >> 2) * 131072;
    m.vl = lane * 16;
    m.oc = id.mt * 128 * 4096 + (id.sp & 1) * 2048;
    m.oab = WS_AB + (id.nt * 4 + (w >> 1)) * 262144 + (id.sp * 64 + (w & 1) * 2) * 1024;
    {
        const int r = lane & 31, h = lane >> 5;
#pragma unroll
        for (int p = 0; p < 4; ++p) {
            const int sw16 = ((2 * p + h) ^ ((r >> 1) & 7)) << 4;
            ps.xa[p] = lds + (32 * (w & 3) + r) * 128 + sw16;
            ps.ra[p] = lds + 16384 + (w >> 2) * 4096 + r * 128 + sw16;
            PIN(ps.xa[p]); PIN(ps.ra[p]);
        }
        const int wl = (8 * h + ((lane & 15) >> 2)) * 64 + ((lane >> 4) & 1) * 32 + (lane & 3) * 8;
        ps.va = lds + 24576 + wl; ps.wa = lds + P_W1 + (w >> 2) * 1024 + wl;
        PIN(ps.va); PIN(ps.wa); PIN(m.vc); PIN(m.vl);
        if (w < 4) asm volatile("s_waitcnt vmcnt(%0)" ::"i"(2 * ncopy(0)) : "memory"); else asm volatile("s_waitcnt vmcnt(%0)" ::"i"(2 * ncopy(1)) : "memory");
        asm volatile("" : "+v"(gx), "+v"(gv), "+v"(gen));
        m.epoch = gen + 1;
        float gate = 0.f;
#pragma unroll
        for (int q = 0; q < 4; ++q) gate += bf_lo(gx[q]) * bf_lo(gv[q]) + bf_hi(gx[q]) * bf_hi(gv[q]);
        gate = dpp_sum<0x111, 0xf>(gate); gate = dpp_sum<0x112, 0xf>(gate); gate = dpp_sum<0x114, 0xf>(gate); gate = dpp_sum<0x118, 0xf>(gate);
        gate = dpp_sum<0x142, 0xa>(gate); gate = dpp_sum<0x143, 0xc>(gate);
        gate = __builtin_amdgcn_rcpf(1.f + __expf(-gate));
        if (lane == 63) __hip_atomic_store((float*)(ws + WS_G) + id.mi * 128 + id.nj * 8 + w, gate, __ATOMIC_RELAXED, __HIP_MEMORY_SCOPE_AGENT);
    }
    if (w < 4) proj<0>(m, id, ps);
    else proj<1>(m, id, ps);
    const u64 epoch = m.epoch;
    const int r = lane & 31, h = lane >> 5, qg = w & 3, kh = w >> 2, tb = (w >> 1) & 1, eb = w & 1;
    const int head = id.head, qb = id.qb, tt = id.tt, et = id.et;
    m.okv = ((w & 2) ? WS_V : WS_K) + id.head * 65536 + (kh * 32 + (w & 1)) * 1024;
    m.owo = w * 16384 + id.et * 128;
    m.op = WS_P + id.sp * (2u << 20) + (id.mt * 4 + (w >> 1)) * 32768 + (id.nt * 4 + (w & 1) * 2) * 2048;
    m.oq = WS_Q + id.head * 65536 + (id.qb * 8 + qg * 2 + kh) * 1024;
    m.opp = WS_P + kh * (4u << 20) + (id.tt * 2 + tb) * 32768 + (id.et * 2 + eb) * 2048;
    m.og = WS_G + (id.tt * 64 + tb * 32) * 4;
    m.fb0 = id.head >> 1;
    m.fc0 = WS_F + 6144 + ((id.tt >> 1) * 4 + (id.et >> 1)) * 32;
    m.fc1 = WS_F + ((id.tt & 1) * 128 + (id.tt >> 1)) * 8;
    m.fo2 = WS_F + 2048 + (id.nj * 16 + id.mi) * 8;
    m.fo4 = WS_F + 6144 + ((id.mt * 4 + id.nt) * 4 + id.sp) * 8;
    mid_setup(m);
    {
        const unsigned fa = ((id.nt * 4 + ((lane >> 3) & 3)) * 16 + (id.sp & 1) * 8 + (lane & 7)) * 8, fv = ((id.head >> 1) * 16 + id.qb * 2 + (lane & 1)) * 8;
        const unsigned polla = WS_F + (lane < 32 ? fa : fv);
        if (w == 7) poll_issue(m.pv, m.sw, polla);
        if (w == 7) wait_vm<1>(); else wait_vm<0>();
        bar();
        if (threadIdx.x == 0) post_flag(ws, m.fo2, epoch);
        if (w == 7) { wait_vm<0>(); poll_finish(m.pv, m.sw, polla, epoch); }
        bar();
    }
#pragma unroll
    for (int st = 0; st < 3; ++st) {
        dma16c(m.l0 + M_AB + st * 16384 + w * 2048, m.vl, m.sw, m.oab + st * 4096);
        dma16c(m.l0 + M_AB + st * 16384 + w * 2048 + 1024, m.vl, m.sw, m.oab + st * 4096 + 1024);
        if (st == 0) bar();
    }
    asm volatile("buffer_load_dwordx4 %0, %2, %3, %4 offen sc1\n\tbuffer_load_dwordx4 %1, %2, %3, %5 offen sc1"
                 : "=&v"(m.q0), "=&v"(m.q1) : "v"(m.vl), "s"(m.sw), "s"(m.oq), "s"(m.oq ^ 1024) : "memory");
    wait_bar<6>();
    if (w == 7) poll_issue(m.pv, m.sw, m.pollv);
    m.fa.a0 = ldsfrag(m.aa); m.fa.c0 = ldsfrag(m.ca[0]); m.fa.a1 = ldsfrag(m.aa + 4096);
    m.fa.a2 = ldsfrag(m.aa + 1024); m.fa.c1 = ldsfrag(m.ca[1]); m.fa.a3 = ldsfrag(m.aa + 4096 + 1024);
    mid_stages<0, LAST + 1>(m);
    m.o1 = pv_mfma<8>(m.fb.v1, m.s1, m.o1, m.idx);

    {
        const f32x16 zero = {};
        const lds_ptr mine = lds + M_X + w * 3072 + lane * 16, other = lds + M_X + (w ^ 4) * 3072 + lane * 16;
#pragma unroll
        for (int j = 0; j < 3; ++j) *(LAS f32x4*)(mine + j * 1024) = f32x4{m.o1[4 * j], m.o1[4 * j + 1], m.o1[4 * j + 2], m.o1[4 * j + 3]};
        wait_bar<0>();
        if (threadIdx.x == 0) post_flag(ws, m.fo4, epoch);
        f32x16 o = m.o0;
#pragma unroll
        for (int j = 0; j < 3; ++j) {
            const f32x4 t = *(const LAS f32x4*)(other + j * 1024);
#pragma unroll
            for (int e = 0; e < 4; ++e) o[4 * j + e] += t[e];
        }
        bool ok = o[8] >= 8.271806e-25f && o[8] <= 1.2676506e30f;
#pragma unroll
        for (int e = 0; e < 8; ++e) ok = ok && (__builtin_fabsf(o[e]) <= 3.0e38f);
        if (__builtin_expect(__any(!ok), 0)) {
            const unsigned kb = WS_K + head * 65536, vb = WS_V + head * 65536, vv = h * 256 + (r & 15) * 16;
            float mx = -__builtin_inff();
            for (int t = 0; t < 64; ++t) {
                const u32x4 kf = __builtin_amdgcn_raw_buffer_load_b128(m.rw, m.vl, kb + t * 1024, 16);
                const f32x16 s = MFMA(__builtin_bit_cast(bf16x8, kf), m.q0, zero);
#pragma unroll
                for (int e = 0; e < 16; ++e) mx = __builtin_fmaxf(mx, s[e]);
            }
            mx = __builtin_fmaxf(mx, __shfl_xor(mx, 32));
            const unsigned cw = (r == 16 || r == 20) ? 0x3f803f80u : 0u;
            const u32x4 cv = {cw, cw, cw, cw};
            o = zero;
            for (int t = 0; t < 64; ++t) {
                const u32x4 kf = __builtin_amdgcn_raw_buffer_load_b128(m.rw, m.vl, kb + t * 1024, 16);
                const u32x4 v0 = __builtin_amdgcn_raw_buffer_load_b128(m.rw, vv, vb + t * 1024, 16);
                const u32x4 v1 = __builtin_amdgcn_raw_buffer_load_b128(m.rw, vv, vb + t * 1024 + 512, 16);
                f32x16 s = MFMA(__builtin_bit_cast(bf16x8, kf), m.q0, zero);
#pragma unroll
                for (int e = 0; e < 16; ++e) s[e] = __builtin_amdgcn_exp2f(s[e] - mx);
                o = pv_mfma<0>(__builtin_bit_cast(bf16x8, r < 16 ? v0 : cv), s, o, m.idx);
                o = pv_mfma<8>(__builtin_bit_cast(bf16x8, r < 16 ? v1 : cv), s, o, m.idx);
            }
        }
        store16c(pack8<0>(o, __builtin_amdgcn_rcpf(o[8])), m.sw, m.vl, WS_O + (qb * 8 + qg * 2 + kh) * 32768 + head * 1024);
    }
    wait_bar<0>();
    if (threadIdx.x == 0) post_flag(ws, WS_F + 4096 + (qb * 32 + head) * 8, epoch);
    if (w == 7) poll_block(m.sw, (lane & 8) ? m.fc0 + (lane & 3) * 8 : m.fc1 + (lane & 7) * 128, epoch);
    bar();
    asm volatile("buffer_load_dwordx4 %0, %5, %6, %7 offen sc1\n\tbuffer_load_dwordx4 %1, %5, %6, %7 offen offset:1024 sc1\n\t"
                 "buffer_load_dwordx4 %2, %5, %6, %8 offen sc1\n\tbuffer_load_dwordx4 %3, %5, %6, %8 offen offset:1024 sc1\n\t"
                 "buffer_load_dword %4, %9, %6, %10 offen sc1"
                 : "=&v"(m.p0), "=&v"(m.p1), "=&v"(m.p2), "=&v"(m.p3), "=&v"(m.gate)
                 : "v"(m.vl), "s"(m.sw), "s"(m.opp), "s"(m.opp + (2u << 20)), "v"(r * 4), "s"(m.og) : "memory");
    if (blockIdx.x == 0 && w < 4) poll_block(m.sw, WS_F + 2048 + threadIdx.x * 8, epoch);
    if (w == 7) poll_block(m.sw, WS_F + 4096 + ((tt >> 2) * 32 + r) * 8, epoch);
    bar();

#pragma unroll
    for (int c = 0; c < 4; ++c) {
        const int ks = c * 4 + (w & 3);
        dma16c(m.l0 + (kh ? o_chunk(1) : o_chunk(0)) + ks * 1024, m.vl, m.sw, WS_O + (tt * 2) * 32768 + (kh * 16 + ks) * 1024);
        dma16c(m.l0 + (kh ? o_chunk(3) : o_chunk(2)) + ks * 1024, m.vl, m.sw, WS_O + (tt * 2 + 1) * 32768 + (kh * 16 + ks) * 1024);
    }
    {
        const int q = (lane & 15) >> 2;
        const lds_ptr oa = lds + (tb ? (kh ? o_chunk(3) : o_chunk(2)) : (kh ? o_chunk(1) : o_chunk(0))) + lane * 16;
        const int wl = (4 * h + q) * 128 + ((eb * 64) ^ ((q & 2) << 5)) + ((lane >> 4) & 1) * 32 + (lane & 3) * 8;
        const lds_ptr wa0 = lds + (kh ? wo_chunk(2) : wo_chunk(0)) + wl, wa1 = lds + (kh ? wo_chunk(3) : wo_chunk(1)) + wl;
        bf16x8 wf[16];
#pragma unroll
        for (int ks = 0; ks < 16; ++ks) {
            const lds_ptr p = (ks < 8 ? wa0 : wa1) + (ks & 7) * 2048;
            wf[ks] = ldsfrag_tr(p, p + 1024);
        }
        asm volatile("s_waitcnt vmcnt(8)" : "+v"(m.p0), "+v"(m.p1), "+v"(m.p2), "+v"(m.p3), "+v"(m.gate)::"memory");
        f32x16 yv;
#pragma unroll
        for (int j = 0; j < 8; ++j) {
            const unsigned a = j < 4 ? m.p0[j & 3] : m.p1[j & 3], b = j < 4 ? m.p2[j & 3] : m.p3[j & 3];
            yv[2 * j] = m.gate * (bf_lo(a) + bf_lo(b));
            yv[2 * j + 1] = m.gate * (bf_hi(a) + bf_hi(b));
        }
        PIN(yv);
        f32x16 acc = {};
#pragma unroll
        for (int c = 0; c < 4; ++c) {
            SB();
            if (c == 0) wait_bar<6>(); else if (c == 1) wait_bar<4>(); else if (c == 2) wait_bar<2>(); else wait_bar<0>();
            bf16x8 of[4];
#pragma unroll
            for (int p = 0; p < 4; ++p) of[p] = ldsfrag(oa + (c * 4 + p) * 1024);
            SB();
#pragma unroll
            for (int p = 0; p < 4; ++p) acc = MFMA(wf[c * 4 + p], of[p], acc);
        }
        const float og = 1.f - m.gate;
        const lds_ptr st = lds + 16384 + kh * M_AB + (tb * 32 + r) * O_PITCH + (eb * 32 + 4 * h) * 4;
#pragma unroll
        for (int j = 0; j < 4; ++j) {
            const f32x4 v = {yv[4 * j] + og * acc[4 * j], yv[4 * j + 1] + og * acc[4 * j + 1], yv[4 * j + 2] + og * acc[4 * j + 2], yv[4 * j + 3] + og * acc[4 * j + 3]};
            *(LAS f32x4*)(st + j * 32) = v;
        }
        asm volatile("s_waitcnt lgkmcnt(0)\n\ts_barrier" ::: "memory");
        const int row = threadIdx.x >> 3, ch = threadIdx.x & 7;
        const lds_ptr rd = lds + 16384 + row * O_PITCH + ch * 32;
        const f32x4 a0 = *(const LAS f32x4*)rd, a1 = *(const LAS f32x4*)(rd + 16);
        const f32x4 b0 = *(const LAS f32x4*)(rd + M_AB), b1 = *(const LAS f32x4*)(rd + M_AB + 16);
        const u32x4 o = {pk2(a0[0] + b0[0], a0[1] + b0[1]), pk2(a0[2] + b0[2], a0[3] + b0[3]), pk2(a1[0] + b1[0], a1[1] + b1[1]), pk2(a1[2] + b1[2], a1[3] + b1[3])};
        asm volatile("global_store_dwordx4 %0, %1, off sc1\n\ts_nop 1" ::"v"(out + (size_t)(tt * 64 + row) * 1024 + et * 128 + ch * 16), "v"(o) : "memory");
    }
    asm volatile(".Lpc1:");
    if (blockIdx.x == 0 && threadIdx.x == 0) post_flag(ws, WS_GEN, epoch);
}

extern "C" void kernel_launch(void* const* d_in, const int* in_sizes, int n_in, void* d_out, int out_size, void* d_ws, size_t ws_size, hipStream_t stream) {
    (void)in_sizes; (void)n_in; (void)out_size; (void)ws_size;
    (void)hipFuncSetAttribute((const void*)k_fused, hipFuncAttributeMaxDynamicSharedMemorySize, LDS_BYTES);
    k_fused<<<256, 512, LDS_BYTES, stream>>>((const char*)d_in[0], (const char*)d_in[1], (const char*)d_in[2], (const char*)d_in[3], (const char*)d_in[4],
                                             (const char*)d_in[6], (const char*)d_in[7], (const char*)d_in[8], (char*)d_ws, (char*)d_out);
}
```
